```python
import torch

def _softmax(x, axis=-1):
    return torch.softmax(x, dim=axis)
def _gelu(x):
    return torch.nn.functional.gelu(x, approximate="tanh")
def _silu(x):
    return torch.nn.functional.silu(x)
def _sigmoid(x):
    return torch.sigmoid(x)
def _layernorm(x, axis=-1, eps=1e-5):
    m = x.mean(dim=axis, keepdim=True)
    v = x.var(dim=axis, keepdim=True, unbiased=False)
    return (x - m) / torch.sqrt(v + eps)
def _scatter_add(tgt, idx, src, axis=0):
    return tgt.clone().index_add_(axis, idx, src)
def _segment_sum(src, seg, n):
    out = torch.zeros((n,) + src.shape[1:], dtype=src.dtype)
    return out.index_add_(0, seg, src)
def _pad(x, pw):
    flat = tuple(v for pair in reversed(pw) for v in pair)
    return torch.nn.functional.pad(x, flat)

kernel_name = 'parallel_sum__ssm_conv_s4d__swiglu_ffn__prompt'

def setup_inputs():

    _w = (lambda a: (lambda t: t.to(torch.bfloat16) if t.is_floating_point() else t)(torch.as_tensor(a)))
    def _A_setup():
        Bsz, L, D, N = 1, 2048, 512, 128
        return {
            "u": _w((lambda *s: torch.randn(*s, dtype=torch.bfloat16) * 0.05)(Bsz, L, D)),
            "log_A_real": _w((lambda *s: torch.randn(*s, dtype=torch.bfloat16) * 3.2)(D, N) * 0.5),
            "A_imag": _w((lambda *s: torch.randn(*s, dtype=torch.bfloat16) * 0.1)(D, N) * 3.14),
            "C_real": _w((lambda *s: torch.randn(*s, dtype=torch.bfloat16) * 1.6)(D, N) / N ** 0.5),
            "C_imag": _w((lambda *s: torch.randn(*s, dtype=torch.bfloat16) * 1.6)(D, N) / N ** 0.5),
            "log_dt": _w((lambda *s: torch.randn(*s, dtype=torch.bfloat16) * 0.1)(D) * 0.5 - 4.0),
            "D_skip": _w((lambda *s: torch.randn(*s, dtype=torch.bfloat16) * 0.2)(D)),
        }
    _A_in = _A_setup()

    def _B_setup():
        B, S, D, F = 1, 2048, 512, 1792
        return {
            "x": _w((lambda *s: torch.randn(*s, dtype=torch.bfloat16) * 0.05)(B, S, D)),
            "w_gate": _w((lambda *s: torch.randn(*s, dtype=torch.bfloat16) * 0.05)(D, F)),
            "w_up": _w((lambda *s: torch.randn(*s, dtype=torch.bfloat16) * 0.05)(D, F)),
            "w_down": _w((lambda *s: torch.randn(*s, dtype=torch.bfloat16) * 0.05)(F, D)),
        }
    _B_in = _B_setup()

    return {
        "A_u": _A_in["u"],
        "A_log_A_real": _A_in["log_A_real"],
        "A_A_imag": _A_in["A_imag"],
        "A_C_real": _A_in["C_real"],
        "A_C_imag": _A_in["C_imag"],
        "A_log_dt": _A_in["log_dt"],
        "A_D_skip": _A_in["D_skip"],
        "B_x": _B_in["x"],
        "B_w_gate": _B_in["w_gate"],
        "B_w_up": _B_in["w_up"],
        "B_w_down": _B_in["w_down"],
    }

def reference(A_u, A_log_A_real, A_A_imag, A_C_real, A_C_imag, A_log_dt, A_D_skip, B_x, B_w_gate, B_w_up, B_w_down):
    def _A_ref(u, log_A_real, A_imag, C_real, C_imag, log_dt, D_skip):
        L = u.shape[1]
        dt = torch.exp(log_dt)
        A = -torch.exp(log_A_real) + 1j * A_imag
        C = (C_real + 1j * C_imag) * dt[:, None]
        pos = (lambda x, dt: x.to(dt))(torch.arange(L), log_dt.dtype)
        P = torch.exp((A * dt[:, None])[:, :, None] * pos[None, None, :])
        K = 2.0 * torch.einsum("dn,dnl->dl", C, P).real
        L2 = 2 * L
        u_t = (lambda x, *d: x.permute(*d))(u, 0, 2, 1)
        Kf = (lambda x, *a, **k: torch.fft.rfft(x.float(), *a, **k))(K, L2)
        Uf = (lambda x, *a, **k: torch.fft.rfft(x.float(), *a, **k))(u_t, L2)
        y = (lambda x, *a, **k: torch.fft.irfft(x, *a, **k).to(torch.bfloat16))(Uf * Kf[None, :, :], L2)[:, :, :L]
        y = (lambda x, *d: x.permute(*d))(y, 0, 2, 1)
        return y + u * D_skip[None, None, :]

    def _B_ref(x, w_gate, w_up, w_down):
        g = _silu(x @ w_gate)
        u = x @ w_up
        return (g * u) @ w_down

    x = A_u
    xn = _layernorm(x, axis=-1)
    return x + _A_ref(xn, A_log_A_real, A_A_imag, A_C_real, A_C_imag, A_log_dt, A_D_skip) + _B_ref(xn, B_w_gate, B_w_up, B_w_down)
```

```cpp
#include <hip/hip_runtime.h>
#include <hip/hip_bf16.h>
#include <stdint.h>

typedef __attribute__((ext_vector_type(8))) __bf16 bf16x8;
typedef __attribute__((ext_vector_type(2))) __bf16 bf16x2;
typedef __attribute__((ext_vector_type(8))) _Float16 f16x8;
typedef __attribute__((ext_vector_type(2))) _Float16 f16x2;
typedef __attribute__((ext_vector_type(4))) short s16x4;
typedef __attribute__((ext_vector_type(2))) float f32x2;
typedef __attribute__((ext_vector_type(4))) float f32x4;
typedef __attribute__((ext_vector_type(16))) float f32x16;
typedef __attribute__((ext_vector_type(4))) unsigned u32x4;
typedef __attribute__((ext_vector_type(2))) unsigned u32x2;
#define LDSP(T, a) ((__attribute__((address_space(3))) T*)(a))
#define GLB(T, a) ((__attribute__((address_space(1))) T*)(a))

constexpr int L_ = 2048, D_ = 512, F_ = 1792;
constexpr size_t WS_H = 0;
constexpr size_t WS_WD = WS_H + 64ull * 112 * 1024;
constexpr size_t WS_UT = WS_WD + 16ull * 112 * 1024;
constexpr size_t WS_KR = WS_UT + 512ull * 2048 * 2;
constexpr size_t WS_AT = WS_KR + 512ull * 2 * 2080 * 2;
constexpr size_t WS_KS = WS_AT + 512ull * 2048 * 2;
constexpr size_t WS_CNT = WS_KS + 2048;

__device__ __forceinline__ float bflo(unsigned v) { return __uint_as_float(v << 16); }
__device__ __forceinline__ float bfhi(unsigned v) { return __uint_as_float(v & 0xffff0000u); }
__device__ __forceinline__ float bf1(unsigned short v) { return __uint_as_float((unsigned)v << 16); }
__device__ __forceinline__ unsigned pkbf(float a, float b) {
  f32x2 f = {a, b};
  return __builtin_bit_cast(unsigned, __builtin_convertvector(f, bf16x2));
}
__device__ __forceinline__ float rbf(float a) { return bflo(pkbf(a, a)); }
__device__ __forceinline__ unsigned pkh(float a, float b) {
  f32x2 f = {a, b};
  return __builtin_bit_cast(unsigned, __builtin_convertvector(f, f16x2));
}
__device__ __forceinline__ void glds16(const void* g, unsigned lds_dst) {
  unsigned keep;
  asm volatile("s_mov_b32 %0, m0\n\ts_mov_b32 m0, %2\n\ts_nop 0\n\tglobal_load_lds_dwordx4 %1, off\n\ts_mov_b32 m0, %0"
               : "=&s"(keep) : "v"(g), "s"(lds_dst) : "memory");
}
#define WAITV(n) asm volatile("s_waitcnt vmcnt(" #n ")" ::: "memory")
#define BAR() asm volatile("s_waitcnt lgkmcnt(0)\n\ts_barrier" ::: "memory")

struct cplx { float r, i; };
__device__ __forceinline__ cplx cmul(cplx a, cplx b) { return {a.r * b.r - a.i * b.i, a.r * b.i + a.i * b.r}; }
__device__ __forceinline__ cplx cexpz(float zr, float zi, float p) {
  float e = __builtin_amdgcn_exp2f(zr * p * 1.44269504088896f);
  float th = __builtin_amdgcn_fractf(zi * p * 0.15915494309189535f);
  return {e * __builtin_amdgcn_cosf(th), e * __builtin_amdgcn_sinf(th)};
}

constexpr int ST_BYTES = 64 * 448;
constexpr int K1_LDS = 5 * ST_BYTES + 4096;
constexpr int KG_OFF = 2 * ST_BYTES;
constexpr int KG_ROW = 528;
constexpr int KG_CH = 64 * KG_ROW;
constexpr int KT_OFF = 5 * ST_BYTES;

__global__ void __launch_bounds__(256, 1)
k1(const unsigned short* __restrict__ x, const unsigned short* __restrict__ lar, const unsigned short* __restrict__ aim,
   const unsigned short* __restrict__ cre, const unsigned short* __restrict__ cim, const unsigned short* __restrict__ ldt,
   const unsigned short* __restrict__ wg, const unsigned short* __restrict__ wu, const unsigned short* __restrict__ wd,
   char* __restrict__ ws) {
  extern __shared__ __attribute__((aligned(16))) char smem[];
  const unsigned sb = (unsigned)(uintptr_t)LDSP(char, smem);
  const int tid = threadIdx.x, lane = tid & 63, w = __builtin_amdgcn_readfirstlane(tid >> 6);
  const int bid = blockIdx.x, xc = bid & 7, Lc = bid >> 3;
  const int mi = 8 * (xc & 1) + (Lc & 7), fi = 4 * (xc >> 1) + (Lc >> 3);
  const int ch = w >> 1, dch = 2 * bid + ch, n = tid & 127;

  const unsigned short p_lar = lar[dch * 128 + n], p_aim = aim[dch * 128 + n];
  const unsigned short p_cr = cre[dch * 128 + n], p_ci = cim[dch * 128 + n];
  const unsigned short p_cr2 = cre[dch * 128 + (n ^ 64)], p_ci2 = cim[dch * 128 + (n ^ 64)];
  const unsigned short p_dt = ldt[dch];
  u32x4 xr[32];
  {
    const char* xb = (const char*)x + (size_t)(mi * 128 + w * 32 + (lane & 15)) * 1024 + (lane >> 4) * 16;
#pragma unroll
    for (int g = 0; g < 16; ++g) {
      xr[2 * g] = *(const u32x4*)(xb + g * 64);
      xr[2 * g + 1] = *(const u32x4*)(xb + 16 * 1024 + g * 64);
    }
  }
  const char* wsrc[7];
#pragma unroll
  for (int i = 0; i < 7; ++i) {
    int U = (w * 7 + i) * 64 + lane, k = U / 28, c = U - k * 28;
    const char* base = (const char*)(c < 14 ? wg : wu);
    int cc = c < 14 ? c : c - 14;
    wsrc[i] = base + (size_t)k * (F_ * 2) + fi * 224 + cc * 16;
  }
  auto stage = [&](int st, int slot) {
#pragma unroll
    for (int i = 0; i < 7; ++i)
      glds16(wsrc[i] + (size_t)st * (64 * F_ * 2), sb + slot * ST_BYTES + (w * 7 + i) * 1024);
  };

  {
    float dt = rbf(__expf(bf1(p_dt)));
    float zr = -rbf(__expf(bf1(p_lar))) * dt, zi = bf1(p_aim) * dt;
    float cr = bf1(p_cr) * dt, ci = bf1(p_ci) * dt;
    float cm = fmaxf(fmaxf(fabsf(cr), fabsf(ci)), fmaxf(fabsf(bf1(p_cr2) * dt), fabsf(bf1(p_ci2) * dt)));
    asm volatile("" : "+v"(cm));
    stage(0, 0);
    stage(1, 1);
#pragma unroll
    for (int o = 32; o; o >>= 1) cm = fmaxf(cm, __shfl_xor(cm, o));
    float inv = cm > 0.f ? 1.0f / cm : 0.f;
    if ((tid & 127) == 0) *(float*)(ws + WS_KS + dch * 4) = 2.0f * cm;
    if (bid == 0 && tid < 8) *(unsigned*)(ws + WS_CNT + tid * 256) = 0u;
    char* kg = smem + KG_OFF + ch * KG_CH + n * 4;
    auto putA = [&](int row, cplx v) { *(unsigned*)(kg + row * KG_ROW) = pkh(v.r, v.i); };
    auto putB = [&](int row, cplx v) { *(unsigned*)(kg + (48 + row) * KG_ROW) = pkh(v.r, -v.i); };
    cplx e1 = cexpz(zr, zi, 1.f), e8 = cexpz(zr, zi, 8.f), e128 = cexpz(zr, zi, 128.f);
    cplx R[8];
    R[1] = e1;
#pragma unroll
    for (int r = 2; r < 8; ++r) R[r] = cmul(R[r - 1], e1);
    cplx W = {1.f, 0.f};
    putB(0, W);
    W = e8;
    putB(1, W);
#pragma unroll
    for (int b = 2; b < 16; ++b) { W = cmul(W, e8); putB(b, W); }
    cplx G = {cr * inv, ci * inv};
#pragma unroll
    for (int q = 0; q <= 16; ++q) {
      if (q < 2) {
        putA(8 * q, G);
#pragma unroll
        for (int r = 1; r < 8; ++r) putA(8 * q + r, cmul(G, R[r]));
      } else if (q < 4) {
        putA(16 + 4 * (q - 2), G);
#pragma unroll
        for (int r = 1; r < 4; ++r) putA(16 + 4 * (q - 2) + r, cmul(G, R[2 * r]));
      } else if (q < 8) {
        putA(24 + 2 * (q - 4), G);
        putA(24 + 2 * (q - 4) + 1, cmul(G, R[4]));
      } else {
        putA(24 + q, G);
      }
      if (q < 16) G = cmul(G, e128);
    }
    BAR();
    const char* kb = smem + KG_OFF + ch * KG_CH + (lane & 15) * KG_ROW + (lane >> 4) * 16;
    const int t0 = (w & 1) ? 2 : 0;
    f32x4 a0 = {0, 0, 0, 0}, a1 = {0, 0, 0, 0};
#pragma unroll
    for (int ks = 0; ks < 8; ++ks) {
      f16x8 bfr = *(const f16x8*)(kb + 48 * KG_ROW + ks * 64);
      f16x8 af0 = *(const f16x8*)(kb + t0 * 16 * KG_ROW + ks * 64);
      f16x8 af1 = *(const f16x8*)(kb + 16 * KG_ROW + ks * 64);
      a0 = __builtin_amdgcn_mfma_f32_16x16x32_f16(af0, bfr, a0, 0, 0, 0);
      a1 = __builtin_amdgcn_mfma_f32_16x16x32_f16(af1, bfr, a1, 0, 0, 0);
    }
    _Float16* T = (_Float16*)(smem + KT_OFF) + ch * 768;
    const int b = lane & 15;
#pragma unroll
    for (int r = 0; r < 4; ++r) {
      int i0 = (lane >> 4) * 4 + r;
      if (w & 1) {
        if (i0 < 8 || (i0 == 8 && b == 0)) T[384 + 16 * (8 + i0) + b] = (_Float16)a0[r];
      } else {
        T[(i0 & 7) + 128 * (i0 >> 3) + 8 * b] = (_Float16)a0[r];
        int cidx = i0 < 8 ? 128 + 128 + (i0 & 3) + 64 * (i0 >> 2) + 4 * b
                          : 256 + 128 + (i0 & 1) + 32 * ((i0 - 8) >> 1) + 2 * b;
        T[cidx] = (_Float16)a1[r];
      }
    }
    BAR();
#pragma unroll
    for (int c2 = 0; c2 < 2; ++c2) {
      const unsigned short* Tc = (const unsigned short*)(smem + KT_OFF) + c2 * 768;
      unsigned v[9];
#pragma unroll
      for (int j = 0; j < 9; ++j) {
        int l = 2047 - (8 * tid + j);
        int lc = l < 0 ? 0 : l;
        int sh = max(24 - __clz(lc | 1), 0);
        int rr = (lc + (((1 << sh) >> 1) - 1) + ((lc >> sh) & 1)) >> sh;
        rr = sh ? rr : lc;
        unsigned val = Tc[rr + 128 * sh];
        v[j] = l < 0 ? 0u : val;
      }
      u32x4 e = {v[0] | v[1] << 16, v[2] | v[3] << 16, v[4] | v[5] << 16, v[6] | v[7] << 16};
      u32x4 o = {v[1] | v[2] << 16, v[3] | v[4] << 16, v[5] | v[6] << 16, v[7] | v[8] << 16};
      char* kr = ws + WS_KR + (size_t)(2 * bid + c2) * (2 * 4160);
      *(u32x4*)(kr + tid * 16) = e;
      *(u32x4*)(kr + 4160 + tid * 16) = o;
    }
    if (tid < 16) {
      u32x4 z = {0, 0, 0, 0};
      *(u32x4*)(ws + WS_KR + (size_t)(4 * bid + (tid >> 2)) * 4160 + 4096 + (tid & 3) * 16) = z;
    }
  }

  for (int fr = bid * 7 + w; fr < bid * 7 + 7; fr += 4) {
    int dt = fr / 112, ks = fr - dt * 112;
    const unsigned short* s = wd + (size_t)(16 * ks + 4 * (lane >> 5)) * D_ + 32 * dt + (lane & 31);
    unsigned v[8];
#pragma unroll
    for (int j = 0; j < 8; ++j) v[j] = s[(8 * (j >> 2) + (j & 3)) * D_];
    u32x4 o = {v[0] | v[1] << 16, v[2] | v[3] << 16, v[4] | v[5] << 16, v[6] | v[7] << 16};
    *(u32x4*)(ws + WS_WD + (size_t)fr * 1024 + lane * 16) = o;
  }

  float mean, mb;
  {
    float s0 = 0.f, s1 = 0.f;
    const bf16x2 one = {(__bf16)1.0f, (__bf16)1.0f};
#pragma unroll
    for (int g = 0; g < 16; ++g) {
#pragma unroll
      for (int e = 0; e < 4; ++e) {
        u32x2 r = __builtin_amdgcn_permlane16_swap(xr[2 * g][e], xr[2 * g + 1][e], false, false);
        const unsigned r0 = r[0], r1 = r[1];
        xr[2 * g][e] = r0;
        xr[2 * g + 1][e] = r1;
        s0 = __builtin_amdgcn_fdot2_f32_bf16(__builtin_bit_cast(bf16x2, r0), one, s0, false);
        s1 = __builtin_amdgcn_fdot2_f32_bf16(__builtin_bit_cast(bf16x2, r1), one, s1, false);
      }
    }
    s0 += s1;
    s0 += __shfl_xor(s0, 32);
    mean = s0 * (1.0f / 512.0f);
    mb = rbf(mean);
  }
  const u32x4* xs = (const u32x4*)(x + (size_t)(mi * 128 + w * 32 + (lane & 31)) * D_ + 32 * fi + 16 * (lane >> 5));
  const u32x4 xu0 = xs[0], xu1 = xs[1];

  WAITV(0);
  BAR();
  stage(2, 2);
  stage(3, 3);
  stage(4, 4);

  f32x16 acc[7];
#pragma unroll
  for (int j = 0; j < 7; ++j)
#pragma unroll
    for (int r = 0; r < 16; ++r) acc[j][r] = 0.f;
  float q0 = 0.f, q1 = 0.f;
  const unsigned tra = sb + (16 * (lane >> 5) + ((lane >> 2) & 3)) * 448 + ((lane >> 4) & 1) * 224 + (lane & 3) * 8;
#pragma unroll
  for (int st = 0; st < 8; ++st) {
    if (st == 2) WAITV(21);
    if (st == 3) WAITV(21);
    if (st == 4) WAITV(21);
    if (st == 5) WAITV(14);
    if (st == 6) WAITV(7);
    if (st == 7) WAITV(0);
    if (st > 0) BAR();
    if (st >= 1 && st <= 3) stage(st + 4, (st - 1));
    const unsigned base = tra + (st % 5) * ST_BYTES;
#pragma unroll
    for (int s4 = 0; s4 < 4; ++s4) {
      const int s = st * 4 + s4;
      u32x4 d;
#pragma unroll
      for (int e = 0; e < 4; ++e) {
        float a = bflo(xr[s][e]) - mb, b = bfhi(xr[s][e]) - mb;
        q0 = fmaf(a, a, q0);
        q1 = fmaf(b, b, q1);
        d[e] = pkbf(a, b);
      }
      const bf16x8 bfr = __builtin_bit_cast(bf16x8, d);
      const unsigned ko = ((s4 >> 1) * 32 + (s4 & 1) * 8) * 448;
#pragma unroll
      for (int j = 0; j < 7; ++j) {
        s16x4 lo = __builtin_amdgcn_ds_read_tr16_b64_v4i16(LDSP(s16x4, base + ko + j * 32));
        s16x4 hi = __builtin_amdgcn_ds_read_tr16_b64_v4i16(LDSP(s16x4, base + ko + 4 * 448 + j * 32));
        bf16x8 afr = __builtin_bit_cast(bf16x8, __builtin_shufflevector(lo, hi, 0, 1, 2, 3, 4, 5, 6, 7));
        acc[j] = __builtin_amdgcn_mfma_f32_32x32x16_bf16(afr, bfr, acc[j], 0, 0, 0);
      }
    }
  }
  q0 += q1;
  q0 += __shfl_xor(q0, 32);
  const float dm = mean - mb;
  const float var = fmaxf(q0 * (1.0f / 512.0f) - dm * dm, 0.f);
  const float sd = rbf(sqrtf(rbf(rbf(var) + 1e-5f)));
  const float rs = 1.0f / sd;
  {
    const float c1 = -rs * 1.44269504088896f, s2 = sd * sd;
    char* hp = ws + WS_H + ((size_t)(mi * 4 + w) * 112 + fi * 7) * 1024 + lane * 16;
#pragma unroll
    for (int j = 0; j < 7; ++j) {
      float hv[8];
#pragma unroll
      for (int r = 0; r < 8; ++r) {
        float g = acc[j][r], u = acc[j][r + 8];
        float e = __builtin_amdgcn_exp2f(g * c1);
        hv[r] = g * u * __builtin_amdgcn_rcpf(fmaf(e, s2, s2));
      }
      u32x4 o = {pkbf(hv[0], hv[1]), pkbf(hv[2], hv[3]), pkbf(hv[4], hv[5]), pkbf(hv[6], hv[7])};
      *(u32x4*)(hp + j * 1024) = o;
    }
  }
  BAR();
  {
    _Float16* tl = (_Float16*)smem;
    const int tcol = w * 32 + (lane & 31), dr = 16 * (lane >> 5);
#pragma unroll
    for (int e = 0; e < 4; ++e) {
      tl[(dr + 2 * e) * 136 + tcol] = (_Float16)rbf(rbf(bflo(xu0[e]) - mb) / sd);
      tl[(dr + 2 * e + 1) * 136 + tcol] = (_Float16)rbf(rbf(bfhi(xu0[e]) - mb) / sd);
      tl[(dr + 8 + 2 * e) * 136 + tcol] = (_Float16)rbf(rbf(bflo(xu1[e]) - mb) / sd);
      tl[(dr + 8 + 2 * e + 1) * 136 + tcol] = (_Float16)rbf(rbf(bfhi(xu1[e]) - mb) / sd);
    }
    BAR();
#pragma unroll
    for (int i = 0; i < 2; ++i) {
      int c = tid + 256 * i, dd = c >> 4, u = c & 15;
      u32x4 v = *(const u32x4*)(smem + dd * 272 + u * 16);
      *(u32x4*)(ws + WS_UT + (size_t)(32 * fi + dd) * 4096 + mi * 256 + u * 16) = v;
    }
  }
}

constexpr int CV_K = 19 * 1024;
constexpr int CV_U = 4608;
constexpr int CV_W = CV_K + CV_U;
constexpr int RED_OFF = 4 * CV_W;
constexpr int CNT_OFF = RED_OFF + 65536;
constexpr int K2_LDS = CNT_OFF + 64;

__global__ void __launch_bounds__(512, 2)
k2(const unsigned short* __restrict__ x, const unsigned short* __restrict__ dskip, char* __restrict__ ws,
   unsigned short* __restrict__ out) {
  extern __shared__ __attribute__((aligned(16))) char smem[];
  const unsigned sb = (unsigned)(uintptr_t)LDSP(char, smem);
  const int tid = threadIdx.x, lane = tid & 63, w = __builtin_amdgcn_readfirstlane(tid >> 6);
  const int bid = blockIdx.x, xc = bid & 7, Lc = bid >> 3;
  const int tb = 4 * xc + (Lc & 3), db = Lc >> 2;
  if (tid < 4) *(unsigned*)(smem + CNT_OFF + tid * 4) = 0u;
  BAR();
  if (w < 4) {
    const int ch = w >> 1, hw = w & 1, dch = 2 * bid + ch;
    const unsigned kb = sb + w * CV_W, ub = kb + CV_K;
    const char* ug = ws + WS_UT + (size_t)dch * 4096 + lane * 16;
#pragma unroll
    for (int i = 0; i < 4; ++i) glds16(ug + i * 1024, ub + 480 + i * 1024);
    const char* kg = ws + WS_KR + (size_t)dch * (2 * 4160);
#pragma unroll
    for (int i = 0; i < 19; ++i) {
      int S = i * 64 + lane, c = S / 148, r = S - c * 148;
      int m = r / 17, rr = r - m * 17;
      c = c > 7 ? 7 : c;
      m = m > 7 ? 7 : m;
      int U = 240 - 32 * m - 16 * hw + rr;
      glds16(kg + (c & 1) * 4160 + (c & 6) * 2 + U * 16, kb + i * 1024);
    }
    if (lane < 30) { u32x4 z = {0, 0, 0, 0}; *LDSP(u32x4, ub + lane * 16) = z; }
    const float ksc = *(const float*)(ws + WS_KS + dch * 4);
    const float dsk = bf1(dskip[dch]);
    const int b = lane & 15, g = lane >> 4;
    const unsigned aaddr = kb + 16 * ((7 - (b & 7)) * 148 + 13 + g - (b >> 3));
    const unsigned baddr = ub + 16 * (30 - 16 * hw + 2 * b - 2 + g - 12);
    f32x4 acc[8];
#pragma unroll
    for (int i = 0; i < 8; ++i) acc[i] = f32x4{0, 0, 0, 0};
    WAITV(0);
#pragma unroll
    for (int qq = 0; qq < 4; ++qq) {
      f16x8 A[8], B[8];
#pragma unroll
      for (int m = 0; m < 8; ++m) {
        A[m] = *LDSP(f16x8, aaddr + 16 * (17 * m - 4 * qq));
        B[m] = *LDSP(f16x8, baddr + 16 * (32 * m - 4 * qq + 12));
      }
#pragma unroll
      for (int m = 0; m < 8; ++m)
#pragma unroll
        for (int n = 0; n + m < 8; ++n)
          acc[m + n] = __builtin_amdgcn_mfma_f32_16x16x32_f16(A[m], B[n], acc[m + n], 0, 0, 0);
    }
    const unsigned po = b * 64 + g * 16;
#pragma unroll
    for (int i = 0; i < 8; ++i) *LDSP(f32x4, kb + i * 1024 + po) = acc[i];
    asm volatile("s_waitcnt lgkmcnt(0)" ::: "memory");
    unsigned old = 0;
    if (lane == 0) old = __hip_atomic_fetch_add(LDSP(unsigned, sb + CNT_OFF + ch * 4), 1u, __ATOMIC_RELAXED, __HIP_MEMORY_SCOPE_WORKGROUP);
    old = __builtin_amdgcn_readfirstlane(old);
    asm volatile("" ::: "memory");
    if (old == 1) {
      const unsigned ob = sb + (w ^ 1) * CV_W;
      char* ap = ws + WS_AT + (size_t)dch * 4096 + b * 32 + g * 8;
#pragma unroll
      for (int i = 0; i < 8; ++i) {
        f32x4 o = *LDSP(f32x4, ob + i * 1024 + po);
        f32x4 lo = hw ? o : acc[i], hi = hw ? acc[i] : o;
        f32x4 y = (lo + hi) * ksc;
        u32x2 uu = *LDSP(u32x2, ub + 480 + i * 512 + b * 32 + g * 8);
        const unsigned uu0 = uu[0], uu1 = uu[1];
        f16x2 u01 = __builtin_bit_cast(f16x2, uu0), u23 = __builtin_bit_cast(f16x2, uu1);
        float r0 = rbf(y[0]) + rbf((float)u01[0] * dsk), r1 = rbf(y[1]) + rbf((float)u01[1] * dsk);
        float r2 = rbf(y[2]) + rbf((float)u23[0] * dsk), r3 = rbf(y[3]) + rbf((float)u23[1] * dsk);
        unsigned long long pk = (unsigned long long)pkbf(r0, r1) | ((unsigned long long)pkbf(r2, r3) << 32);
        __hip_atomic_store(GLB(unsigned long long, ap + i * 512), pk, __ATOMIC_RELAXED, __HIP_MEMORY_SCOPE_AGENT);
      }
      WAITV(0);
      if (lane == 0)
        __hip_atomic_fetch_add(GLB(unsigned, ws + WS_CNT + (dch >> 6) * 256), 1u, __ATOMIC_RELAXED, __HIP_MEMORY_SCOPE_AGENT);
    }
  } else {
    const int kw = w - 4;
    const u32x4* hp = (const u32x4*)(ws + WS_H + ((size_t)(2 * tb) * 112 + 28 * kw) * 1024) + lane;
    const u32x4* wp = (const u32x4*)(ws + WS_WD + ((size_t)(2 * db) * 112 + 28 * kw) * 1024) + lane;
    f32x16 acc[2][2];
#pragma unroll
    for (int j = 0; j < 2; ++j)
#pragma unroll
      for (int i = 0; i < 2; ++i)
#pragma unroll
        for (int r = 0; r < 16; ++r) acc[j][i][r] = 0.f;
    constexpr int PD = 8;
    u32x4 hb[PD][2], wb[PD][2];
#pragma unroll
    for (int s = 0; s < PD; ++s) {
      hb[s][0] = hp[s * 64]; hb[s][1] = hp[(112 + s) * 64];
      wb[s][0] = wp[s * 64]; wb[s][1] = wp[(112 + s) * 64];
    }
#pragma unroll
    for (int s = 0; s < 28; ++s) {
      const int p = s % PD;
#pragma unroll
      for (int j = 0; j < 2; ++j)
#pragma unroll
        for (int i = 0; i < 2; ++i)
          acc[j][i] = __builtin_amdgcn_mfma_f32_32x32x16_bf16(__builtin_bit_cast(bf16x8, wb[p][j]),
                                                              __builtin_bit_cast(bf16x8, hb[p][i]), acc[j][i], 0, 0, 0);
      if (s + PD < 28) {
        hb[p][0] = hp[(s + PD) * 64]; hb[p][1] = hp[(112 + s + PD) * 64];
        wb[p][0] = wp[(s + PD) * 64]; wb[p][1] = wp[(112 + s + PD) * 64];
      }
      __builtin_amdgcn_sched_barrier(0);
    }
    const unsigned rb = sb + RED_OFF + kw * 16384;
#pragma unroll
    for (int j = 0; j < 2; ++j)
#pragma unroll
      for (int i = 0; i < 2; ++i)
#pragma unroll
        for (int g = 0; g < 4; ++g) {
          const int t = 32 * i + (lane & 31), un = 8 * j + 2 * g + (lane >> 5);
          f32x4 v = {acc[j][i][4 * g], acc[j][i][4 * g + 1], acc[j][i][4 * g + 2], acc[j][i][4 * g + 3]};
          *LDSP(f32x4, rb + t * 256 + ((un ^ (t & 15)) * 16)) = v;
        }
    if (kw == 0) {
      if (lane == 0) {
        unsigned spins = 0;
        while (__hip_atomic_load(GLB(unsigned, ws + WS_CNT + db * 256), __ATOMIC_RELAXED, __HIP_MEMORY_SCOPE_AGENT) < 64u) {
          __builtin_amdgcn_s_sleep(2);
          if (++spins > (1u << 24)) break;
        }
      }
    }
  }
  const int t = tid >> 3, dq = tid & 7;
  const size_t oi = (size_t)(tb * 64 + t) * D_ + db * 64 + dq * 8;
  const u32x4 xv = *(const u32x4*)(x + oi);
  BAR();
  {
    const int dd = tid >> 3, tq = tid & 7;
    const char* ap = ws + WS_AT + (size_t)(db * 64 + dd) * 4096 + tb * 128 + tq * 16;
    unsigned long long a0 = __hip_atomic_load(GLB(unsigned long long, ap), __ATOMIC_RELAXED, __HIP_MEMORY_SCOPE_AGENT);
    unsigned long long a1 = __hip_atomic_load(GLB(unsigned long long, ap + 8), __ATOMIC_RELAXED, __HIP_MEMORY_SCOPE_AGENT);
    unsigned short* tl = (unsigned short*)smem;
#pragma unroll
    for (int e = 0; e < 4; ++e) {
      tl[(tq * 8 + e) * 72 + dd] = (unsigned short)(a0 >> (16 * e));
      tl[(tq * 8 + 4 + e) * 72 + dd] = (unsigned short)(a1 >> (16 * e));
    }
  }
  f32x4 s0 = {0, 0, 0, 0}, s1 = {0, 0, 0, 0};
#pragma unroll
  for (int k = 0; k < 4; ++k) {
    const unsigned rb = sb + RED_OFF + k * 16384 + t * 256;
    s0 += *LDSP(f32x4, rb + (((2 * dq) ^ (t & 15)) * 16));
    s1 += *LDSP(f32x4, rb + (((2 * dq + 1) ^ (t & 15)) * 16));
  }
  BAR();
  const u32x4 av = *(const u32x4*)(smem + t * 144 + dq * 16);
  u32x4 o;
  o[0] = pkbf(rbf(bflo(xv[0]) + bflo(av[0])) + rbf(s0[0]), rbf(bfhi(xv[0]) + bfhi(av[0])) + rbf(s0[1]));
  o[1] = pkbf(rbf(bflo(xv[1]) + bflo(av[1])) + rbf(s0[2]), rbf(bfhi(xv[1]) + bfhi(av[1])) + rbf(s0[3]));
  o[2] = pkbf(rbf(bflo(xv[2]) + bflo(av[2])) + rbf(s1[0]), rbf(bfhi(xv[2]) + bfhi(av[2])) + rbf(s1[1]));
  o[3] = pkbf(rbf(bflo(xv[3]) + bflo(av[3])) + rbf(s1[2]), rbf(bfhi(xv[3]) + bfhi(av[3])) + rbf(s1[3]));
  *(u32x4*)(out + oi) = o;
}

extern "C" void kernel_launch(void* const* d_in, const int* in_sizes, int n_in, void* d_out, int out_size, void* d_ws,
                              size_t ws_size, hipStream_t stream) {
  (void)in_sizes; (void)n_in; (void)out_size; (void)ws_size;
  (void)hipFuncSetAttribute((const void*)k1, hipFuncAttributeMaxDynamicSharedMemorySize, K1_LDS);
  (void)hipFuncSetAttribute((const void*)k2, hipFuncAttributeMaxDynamicSharedMemorySize, K2_LDS);
  typedef const unsigned short* P;
  k1<<<256, 256, K1_LDS, stream>>>((P)d_in[0], (P)d_in[1], (P)d_in[2], (P)d_in[3], (P)d_in[4], (P)d_in[5], (P)d_in[8],
                                   (P)d_in[9], (P)d_in[10], (char*)d_ws);
  k2<<<256, 512, K2_LDS, stream>>>((P)d_in[0], (P)d_in[6], (char*)d_ws, (unsigned short*)d_out);
}
```

```cpp
#include <hip/hip_runtime.h>
#include <hip/hip_bf16.h>
#include <stdint.h>

typedef __attribute__((ext_vector_type(8))) __bf16 bf16x8;
typedef __attribute__((ext_vector_type(2))) __bf16 bf16x2;
typedef __attribute__((ext_vector_type(8))) _Float16 f16x8;
typedef __attribute__((ext_vector_type(2))) _Float16 f16x2;
typedef __attribute__((ext_vector_type(4))) short s16x4;
typedef __attribute__((ext_vector_type(2))) float f32x2;
typedef __attribute__((ext_vector_type(4))) float f32x4;
typedef __attribute__((ext_vector_type(16))) float f32x16;
typedef __attribute__((ext_vector_type(4))) unsigned u32x4;
typedef __attribute__((ext_vector_type(2))) unsigned u32x2;
#define LDSP(T, a) ((__attribute__((address_space(3))) T*)(a))
#define GLB(T, a) ((__attribute__((address_space(1))) T*)(a))

constexpr int L_ = 2048, D_ = 512, F_ = 1792;
constexpr size_t WS_H = 0;
constexpr size_t WS_WD = WS_H + 64ull * 112 * 1024;
constexpr size_t WS_UT = WS_WD + 16ull * 112 * 1024;
constexpr size_t WS_KR = WS_UT + 512ull * 2048 * 2;
constexpr size_t WS_AT = WS_KR + 512ull * 2 * 2080 * 2;
constexpr size_t WS_KS = WS_AT + 512ull * 2048 * 2;
constexpr size_t WS_XN = WS_KS + 2048;

__device__ __forceinline__ float bflo(unsigned v) { return __uint_as_float(v << 16); }
__device__ __forceinline__ float bfhi(unsigned v) { return __uint_as_float(v & 0xffff0000u); }
__device__ __forceinline__ float bf1(unsigned short v) { return __uint_as_float((unsigned)v << 16); }
__device__ __forceinline__ unsigned pkbf(float a, float b) {
  f32x2 f = {a, b};
  return __builtin_bit_cast(unsigned, __builtin_convertvector(f, bf16x2));
}
__device__ __forceinline__ float rbf(float a) { return bflo(pkbf(a, a)); }
__device__ __forceinline__ unsigned pkh(float a, float b) {
  f32x2 f = {a, b};
  return __builtin_bit_cast(unsigned, __builtin_convertvector(f, f16x2));
}
__device__ __forceinline__ void glds16(const void* g, unsigned lds_dst) {
  unsigned keep;
  asm volatile("s_mov_b32 %0, m0\n\ts_mov_b32 m0, %2\n\ts_nop 0\n\tglobal_load_lds_dwordx4 %1, off\n\ts_mov_b32 m0, %0"
               : "=&s"(keep) : "v"(g), "s"(lds_dst) : "memory");
}
#define WAITV(n) asm volatile("s_waitcnt vmcnt(" #n ")" ::: "memory")
#define BAR() asm volatile("s_waitcnt lgkmcnt(0)\n\ts_barrier" ::: "memory")

#define STAMP(k, i)
#define STAMP2(k, i)

__device__ __forceinline__ f32x2 cmul(f32x2 a, f32x2 b) {
  f32x2 ar = {a[0], a[0]}, ai = {a[1], a[1]}, bp = {-b[1], b[0]};
  return __builtin_elementwise_fma(ai, bp, ar * b);
}
__device__ __forceinline__ f32x2 cexpz(float zr, float zi, float p) {
  float e = __builtin_amdgcn_exp2f(zr * p * 1.44269504088896f);
  float th = __builtin_amdgcn_fractf(zi * p * 0.15915494309189535f);
  return f32x2{e * __builtin_amdgcn_cosf(th), e * __builtin_amdgcn_sinf(th)};
}
__device__ __forceinline__ float wsum(float v) {
#pragma unroll
  for (int o = 32; o; o >>= 1) v += __shfl_xor(v, o);
  return v;
}

constexpr int KG_ROW = 528;
constexpr int KG_CH = 64 * KG_ROW;
constexpr int KT_OFF = 2 * KG_CH;
constexpr int K0_LDS = KT_OFF + 3072;

__global__ void __launch_bounds__(256, 2)
k0(const unsigned short* __restrict__ x, const unsigned short* __restrict__ lar, const unsigned short* __restrict__ aim,
   const unsigned short* __restrict__ cre, const unsigned short* __restrict__ cim, const unsigned short* __restrict__ ldt,
   const unsigned short* __restrict__ wd, char* __restrict__ ws) {
  extern __shared__ __attribute__((aligned(16))) char smem[];
  const int tid = threadIdx.x, lane = tid & 63, w = __builtin_amdgcn_readfirstlane(tid >> 6);
  const int bid = blockIdx.x & 255;
  STAMP(0, 0);
  if (blockIdx.x < 256) {
    const int ch = w >> 1, dch = 2 * bid + ch, n = tid & 127;
    const unsigned short p_lar = lar[dch * 128 + n], p_aim = aim[dch * 128 + n];
    const unsigned short p_cr = cre[dch * 128 + n], p_ci = cim[dch * 128 + n];
    const unsigned short p_cr2 = cre[dch * 128 + (n ^ 64)], p_ci2 = cim[dch * 128 + (n ^ 64)];
    const unsigned short p_dt = ldt[dch];
    int idx[9];
    {
      const int l0 = 2040 - 8 * tid;
      const int sh = max(24 - __clz(l0 | 1), 0), bias = sh ? (1 << (sh - 1)) - 1 : 0, om = sh ? 1 : 0;
#pragma unroll
      for (int j = 0; j < 8; ++j) {
        const int l = l0 + 7 - j;
        idx[j] = ((l + bias + ((l >> sh) & om)) >> sh) + 128 * sh;
      }
      const int l = max(l0 - 1, 0);
      const int s9 = max(24 - __clz(l | 1), 0), b9 = s9 ? (1 << (s9 - 1)) - 1 : 0, o9 = s9 ? 1 : 0;
      idx[8] = ((l + b9 + ((l >> s9) & o9)) >> s9) + 128 * s9;
    }
    const float dt = rbf(__expf(bf1(p_dt)));
    const float zr = -rbf(__expf(bf1(p_lar))) * dt, zi = bf1(p_aim) * dt;
    const float cr = bf1(p_cr) * dt, ci = bf1(p_ci) * dt;
    float cm = fmaxf(fmaxf(fabsf(cr), fabsf(ci)), fmaxf(fabsf(bf1(p_cr2) * dt), fabsf(bf1(p_ci2) * dt)));
#pragma unroll
    for (int o = 32; o; o >>= 1) cm = fmaxf(cm, __shfl_xor(cm, o));
    const float inv = cm > 0.f ? 1.0f / cm : 0.f;
    if ((tid & 127) == 0) *(float*)(ws + WS_KS + dch * 4) = 2.0f * cm;
    char* kg = smem + ch * KG_CH + n * 4;
    auto put = [&](int row, f32x2 v) { *(unsigned*)(kg + row * KG_ROW) = pkh(v[0], v[1]); };
    const f32x2 e1 = cexpz(zr, zi, 1.f), e8c = cexpz(zr, -zi, 8.f), e128 = cexpz(zr, zi, 128.f);
    f32x2 R[8];
    R[1] = e1;
#pragma unroll
    for (int r = 2; r < 8; ++r) R[r] = cmul(R[r - 1], e1);
    f32x2 W = {1.f, 0.f};
    put(48, W);
    W = e8c;
    put(49, W);
#pragma unroll
    for (int b = 2; b < 16; ++b) { W = cmul(W, e8c); put(48 + b, W); }
    f32x2 G = {cr * inv, ci * inv};
#pragma unroll
    for (int q = 0; q <= 16; ++q) {
      if (q < 2) {
        put(8 * q, G);
#pragma unroll
        for (int r = 1; r < 8; ++r) put(8 * q + r, cmul(G, R[r]));
      } else if (q < 4) {
        put(16 + 4 * (q - 2), G);
#pragma unroll
        for (int r = 1; r < 4; ++r) put(16 + 4 * (q - 2) + r, cmul(G, R[2 * r]));
      } else if (q < 8) {
        put(24 + 2 * (q - 4), G);
        put(24 + 2 * (q - 4) + 1, cmul(G, R[4]));
      } else {
        put(24 + q, G);
      }
      if (q < 16) G = cmul(G, e128);
    }
    BAR();
    STAMP(0, 1);
    const char* kb = smem + ch * KG_CH + (lane & 15) * KG_ROW + (lane >> 4) * 16;
    const int t0 = (w & 1) ? 2 : 0;
    f32x4 a0 = {0, 0, 0, 0}, a1 = {0, 0, 0, 0};
#pragma unroll
    for (int ks = 0; ks < 8; ++ks) {
      f16x8 bfr = *(const f16x8*)(kb + 48 * KG_ROW + ks * 64);
      f16x8 af0 = *(const f16x8*)(kb + t0 * 16 * KG_ROW + ks * 64);
      f16x8 af1 = *(const f16x8*)(kb + 16 * KG_ROW + ks * 64);
      a0 = __builtin_amdgcn_mfma_f32_16x16x32_f16(af0, bfr, a0, 0, 0, 0);
      a1 = __builtin_amdgcn_mfma_f32_16x16x32_f16(af1, bfr, a1, 0, 0, 0);
    }
    _Float16* T = (_Float16*)(smem + KT_OFF) + ch * 768;
    const int b = lane & 15;
#pragma unroll
    for (int r = 0; r < 4; ++r) {
      int i0 = (lane >> 4) * 4 + r;
      if (w & 1) {
        if (i0 < 8 || (i0 == 8 && b == 0)) T[384 + 16 * (8 + i0) + b] = (_Float16)a0[r];
      } else {
        T[(i0 & 7) + 128 * (i0 >> 3) + 8 * b] = (_Float16)a0[r];
        int cidx = i0 < 8 ? 128 + 128 + (i0 & 3) + 64 * (i0 >> 2) + 4 * b
                          : 256 + 128 + (i0 & 1) + 32 * ((i0 - 8) >> 1) + 2 * b;
        T[cidx] = (_Float16)a1[r];
      }
    }
    BAR();
#pragma unroll
    for (int c2 = 0; c2 < 2; ++c2) {
      const unsigned short* Tc = (const unsigned short*)(smem + KT_OFF) + c2 * 768;
      unsigned v[9];
#pragma unroll
      for (int j = 0; j < 9; ++j) v[j] = Tc[idx[j]];
      if (tid == 255) v[8] = 0u;
      u32x4 e = {v[0] | v[1] << 16, v[2] | v[3] << 16, v[4] | v[5] << 16, v[6] | v[7] << 16};
      u32x4 o = {v[1] | v[2] << 16, v[3] | v[4] << 16, v[5] | v[6] << 16, v[7] | v[8] << 16};
      char* kr = ws + WS_KR + (size_t)(2 * bid + c2) * (2 * 4160);
      *(u32x4*)(kr + tid * 16) = e;
      *(u32x4*)(kr + 4160 + tid * 16) = o;
    }
    if (tid < 16) {
      u32x4 z = {0, 0, 0, 0};
      *(u32x4*)(ws + WS_KR + (size_t)(4 * bid + (tid >> 2)) * 4160 + 4096 + (tid & 3) * 16) = z;
    }
    STAMP(0, 2);
  } else {
    const int r0 = ((bid & 7) * 4 + (bid >> 6)) * 64 + ((bid >> 3) & 7) * 8;
    u32x4 xv[2];
#pragma unroll
    for (int i = 0; i < 2; ++i) xv[i] = *(const u32x4*)(x + (size_t)(r0 + 2 * w + i) * D_ + lane * 8);
    unsigned wv[2][8];
#pragma unroll
    for (int i = 0; i < 2; ++i) {
      const int fr = bid * 7 + min(w + 4 * i, 6), dt = fr / 112, ks = fr - dt * 112;
      const unsigned short* s = wd + (size_t)(16 * ks + 4 * (lane >> 5)) * D_ + 32 * dt + (lane & 31);
#pragma unroll
      for (int j = 0; j < 8; ++j) wv[i][j] = s[(8 * (j >> 2) + (j & 3)) * D_];
    }
    _Float16* tl = (_Float16*)smem;
#pragma unroll
    for (int i = 0; i < 2; ++i) {
      float f[8];
#pragma unroll
      for (int e = 0; e < 4; ++e) { f[2 * e] = bflo(xv[i][e]); f[2 * e + 1] = bfhi(xv[i][e]); }
      const float mean = wsum(((f[0] + f[1]) + (f[2] + f[3])) + ((f[4] + f[5]) + (f[6] + f[7]))) * (1.0f / 512.0f);
      float q = 0.f;
#pragma unroll
      for (int e = 0; e < 8; ++e) q = fmaf(f[e] - mean, f[e] - mean, q);
      const float var = wsum(q) * (1.0f / 512.0f);
      const float mb = rbf(mean), sd = rbf(sqrtf(rbf(rbf(var) + 1e-5f)));
      u32x4 o, oh;
#pragma unroll
      for (int e = 0; e < 4; ++e) {
        const unsigned pk = pkbf(rbf(f[2 * e] - mb) / sd, rbf(f[2 * e + 1] - mb) / sd);
        o[e] = pk;
        oh[e] = pkh(bflo(pk), bfhi(pk));
      }
      const int t = r0 + 2 * w + i;
      *(u32x4*)(ws + WS_XN + ((size_t)(t >> 5) * 32 + (lane >> 1)) * 1024 + ((t & 31) + 32 * (lane & 1)) * 16) = o;
      *(u32x4*)(tl + (2 * w + i) * 512 + lane * 8) = oh;
    }
#pragma unroll
    for (int i = 0; i < 2; ++i) {
      if (w + 4 * i < 7) {
        const unsigned* v = wv[i];
        u32x4 o = {v[0] | v[1] << 16, v[2] | v[3] << 16, v[4] | v[5] << 16, v[6] | v[7] << 16};
        *(u32x4*)(ws + WS_WD + (size_t)(bid * 7 + w + 4 * i) * 1024 + lane * 16) = o;
      }
    }
    BAR();
    {
      unsigned v[8];
#pragma unroll
      for (int r = 0; r < 8; ++r) v[r] = *(const unsigned*)(smem + r * 1024 + tid * 4);
      u32x4 a, b;
#pragma unroll
      for (int e = 0; e < 4; ++e) {
        a[e] = (v[2 * e] & 0xffffu) | (v[2 * e + 1] << 16);
        b[e] = (v[2 * e] >> 16) | (v[2 * e + 1] & 0xffff0000u);
      }
      char* up = ws + WS_UT + (size_t)(2 * tid) * 4096 + r0 * 2;
      *(u32x4*)up = a;
      *(u32x4*)(up + 4096) = b;
    }
  }
}

constexpr int UP_OFF = 64 * 224 + 128;
constexpr int ST_BYTES = UP_OFF + 64 * 224;
constexpr int CV_K = 8 * 4160;
constexpr int CV_C = CV_K + 4608;
constexpr int K1_LDS = 2 * CV_C;

__global__ void __launch_bounds__(256, 2)
k1(const unsigned short* __restrict__ wg, const unsigned short* __restrict__ wu, const unsigned short* __restrict__ dskip,
   char* __restrict__ ws) {
  extern __shared__ __attribute__((aligned(16))) char smem[];
  const unsigned sb = (unsigned)(uintptr_t)LDSP(char, smem);
  const int tid = threadIdx.x, lane = tid & 63, w = __builtin_amdgcn_readfirstlane(tid >> 6);
  const int bid = blockIdx.x & 255;
  if (blockIdx.x < 256) {
    STAMP(1, 0);
    const int xc = bid & 7, Lc = bid >> 3;
    const int mi = 8 * (xc & 1) + (Lc & 7), fi = 4 * (xc >> 1) + (Lc >> 3);
    const char* wbase = (const char*)((w >> 1) ? wu : wg) + fi * 224;
    unsigned woff[7];
#pragma unroll
    for (int i = 0; i < 7; ++i) {
      const int U = (w & 1) * 448 + i * 64 + lane, k = U / 14, c = U - k * 14;
      woff[i] = k * (F_ * 2) + c * 16;
    }
    const unsigned swa = sb + (w >> 1) * UP_OFF + ((w & 1) * 448 + lane) * 16;
    const char* xb = ws + WS_XN + (size_t)(mi * 4 + w) * 32768;
    u32x4 wr[2][7], xr[2][4];
    auto gload = [&](int st, int p, int i) { wr[p][i] = *(const u32x4*)(wbase + (size_t)st * (64 * F_ * 2) + woff[i]); };
    auto xload = [&](int st, int p, int i) { xr[p][i] = *(const u32x4*)(xb + (st * 4 + i) * 1024 + lane * 16); };
    auto swrite = [&](int p, int i) { *LDSP(u32x4, swa + p * ST_BYTES + i * 1024) = wr[p][i]; };
#pragma unroll
    for (int p = 0; p < 2; ++p) {
#pragma unroll
      for (int i = 0; i < 7; ++i) gload(p, p, i);
#pragma unroll
      for (int i = 0; i < 4; ++i) xload(p, p, i);
    }
    f32x16 acc[7];
#pragma unroll
    for (int j = 0; j < 7; ++j)
#pragma unroll
      for (int r = 0; r < 16; ++r) acc[j][r] = 0.f;
#pragma unroll
    for (int p = 0; p < 2; ++p)
#pragma unroll
      for (int i = 0; i < 7; ++i) { swrite(p, i); gload(p + 2, p, i); }
    const unsigned tra = sb + (8 * (lane >> 5) + ((lane >> 2) & 3)) * 224 + ((lane >> 4) & 1) * UP_OFF + (lane & 3) * 8;
    bf16x8 af[4];
    auto aread = [&](int g) {
      const int st = g / 28, m = g - st * 28, s4 = m / 7, j = m - s4 * 7;
      const unsigned o = (st & 1) * ST_BYTES + s4 * 16 * 224 + j * 32;
      s16x4 lo = __builtin_amdgcn_ds_read_tr16_b64_v4i16(LDSP(s16x4, tra + o));
      s16x4 hi = __builtin_amdgcn_ds_read_tr16_b64_v4i16(LDSP(s16x4, tra + o + 4 * 224));
      af[g & 3] = __builtin_bit_cast(bf16x8, __builtin_shufflevector(lo, hi, 0, 1, 2, 3, 4, 5, 6, 7));
    };
    BAR();
    STAMP(1, 1);
    aread(0); aread(1); aread(2);
    __builtin_amdgcn_sched_barrier(0);
#pragma unroll
    for (int st = 0; st < 8; ++st)
#pragma unroll
    for (int m = 0; m < 28; ++m) {
      const int g = st * 28 + m, p = st & 1, s4 = m / 7, j = m - s4 * 7;
      if (m == 26 && st < 7) {
        BAR();
        aread(g + 2);
        aread(g + 3);
      } else if (m < 25 || (m == 27 && st < 7)) {
        aread(g + 3);
      }
      {
        const int wi = m >= 26 ? m - 26 : m + 2, ws_ = m >= 26 ? st : st - 1;
        if (wi < 7 && ws_ >= 0 && ws_ < 6) {
          swrite(ws_ & 1, wi);
          if (ws_ < 4) gload(ws_ + 4, ws_ & 1, wi);
        }
      }
      acc[j] = __builtin_amdgcn_mfma_f32_32x32x16_bf16(af[g & 3], __builtin_bit_cast(bf16x8, xr[p][s4]), acc[j], 0, 0, 0);
      if (j == 6 && st < 6) xload(st + 2, p, s4);
      __builtin_amdgcn_sched_barrier(0);
    }
    STAMP(1, 2);
    char* hp = ws + WS_H + ((size_t)(mi * 4 + w) * 112 + fi * 7) * 1024 + lane * 16;
#pragma unroll
    for (int j = 0; j < 7; ++j) {
      float hv[8];
#pragma unroll
      for (int r = 0; r < 8; ++r) {
        float g = acc[j][r], u = acc[j][r + 8];
        float e = __builtin_amdgcn_exp2f(g * -1.44269504088896f);
        hv[r] = g * u * __builtin_amdgcn_rcpf(1.0f + e);
      }
      u32x4 o = {pkbf(hv[0], hv[1]), pkbf(hv[2], hv[3]), pkbf(hv[4], hv[5]), pkbf(hv[6], hv[7])};
      *(u32x4*)(hp + j * 1024) = o;
    }
    STAMP(1, 3);
  } else {
    STAMP2(1, 0);
    const int ch = w >> 1, hw = w & 1, dch = 2 * bid + ch;
    const unsigned kb = sb + ch * CV_C, ub = kb + CV_K;
    const char* ug = ws + WS_UT + (size_t)dch * 4096 + hw * 2048 + lane * 16;
    glds16(ug, ub + 480 + hw * 2048);
    glds16(ug + 1024, ub + 480 + hw * 2048 + 1024);
    const char* kg = ws + WS_KR + (size_t)dch * (2 * 4160) + hw * 8 + lane * 16;
#pragma unroll
    for (int c = 0; c < 4; ++c)
#pragma unroll
      for (int i = 0; i < 4; ++i)
        glds16(kg + (c & 1) * 4160 + (c & 2) * 2 + i * 1024, kb + (4 * hw + c) * 4160 + i * 1024);
    {
      u32x4 z = {0, 0, 0, 0};
      if (hw == 0 && lane < 30) *LDSP(u32x4, ub + lane * 16) = z;
      if (hw == 1 && lane < 8) *LDSP(u32x4, kb + lane * 4160 + 4096) = z;
    }
    const float ksc = *(const float*)(ws + WS_KS + dch * 4);
    const float dsk = bf1(dskip[dch]);
    const int b = lane & 15, g = lane >> 4;
    const unsigned aaddr = kb + 16 * ((7 - (b & 7)) * 260 + 253 - 224 - 12 - 16 * hw + g - (b >> 3));
    const unsigned baddr = ub + 16 * (30 - 16 * hw + 2 * b - 2 + g - 12);
    f32x4 acc[8];
#pragma unroll
    for (int i = 0; i < 8; ++i) acc[i] = f32x4{0, 0, 0, 0};
    WAITV(0);
    BAR();
    STAMP2(1, 1);
#pragma unroll
    for (int qq = 0; qq < 4; ++qq) {
      f16x8 A[8], B[8];
#pragma unroll
      for (int m = 0; m < 8; ++m) {
        A[m] = *LDSP(f16x8, aaddr + 16 * (224 - 32 * m + 12 - 4 * qq));
        B[m] = *LDSP(f16x8, baddr + 16 * (32 * m - 4 * qq + 12));
      }
#pragma unroll
      for (int m = 0; m < 8; ++m)
#pragma unroll
        for (int n = 0; n + m < 8; ++n)
          acc[m + n] = __builtin_amdgcn_mfma_f32_16x16x32_f16(A[m], B[n], acc[m + n], 0, 0, 0);
    }
    STAMP2(1, 2);
    BAR();
    const unsigned po = b * 64 + g * 16;
    f32x4 mine[4];
#pragma unroll
    for (int i = 0; i < 4; ++i) {
      f32x4 snd;
#pragma unroll
      for (int e = 0; e < 4; ++e) {
        float lo = acc[i][e], hi = acc[4 + i][e];
        asm volatile("" : "+v"(lo), "+v"(hi));
        snd[e] = hw ? lo : hi;
        mine[i][e] = hw ? hi : lo;
      }
      *LDSP(f32x4, kb + (hw * 4 + i) * 1024 + po) = snd;
    }
    BAR();
    char* ap = ws + WS_AT + (size_t)dch * 4096 + hw * 2048 + b * 32 + g * 8;
#pragma unroll
    for (int i = 0; i < 4; ++i) {
      f32x4 o = *LDSP(f32x4, kb + ((hw ^ 1) * 4 + i) * 1024 + po);
      f32x4 y = (mine[i] + o) * ksc;
      u32x2 uu = *LDSP(u32x2, ub + 480 + hw * 2048 + i * 512 + b * 32 + g * 8);
      const unsigned uu0 = uu[0], uu1 = uu[1];
      f16x2 u01 = __builtin_bit_cast(f16x2, uu0), u23 = __builtin_bit_cast(f16x2, uu1);
      float r0 = rbf(y[0]) + rbf((float)u01[0] * dsk), r1 = rbf(y[1]) + rbf((float)u01[1] * dsk);
      float r2 = rbf(y[2]) + rbf((float)u23[0] * dsk), r3 = rbf(y[3]) + rbf((float)u23[1] * dsk);
      u32x2 pk = {pkbf(r0, r1), pkbf(r2, r3)};
      *(u32x2*)(ap + i * 512) = pk;
    }
    STAMP2(1, 3);
  }
}

constexpr int AT_OFF = 8 * 16384;
constexpr int K2_LDS = AT_OFF + 64 * 144;

__global__ void __launch_bounds__(512, 2)
k2(const unsigned short* __restrict__ x, char* __restrict__ ws, unsigned short* __restrict__ out) {
  extern __shared__ __attribute__((aligned(16))) char smem[];
  const unsigned sb = (unsigned)(uintptr_t)LDSP(char, smem);
  const int tid = threadIdx.x, lane = tid & 63, w = __builtin_amdgcn_readfirstlane(tid >> 6);
  const int bid = blockIdx.x, xc = bid & 7, Lc = bid >> 3;
  const int tb = 4 * xc + (Lc & 3), db = Lc >> 2;
  STAMP(2, 0);
  const u32x4* hp = (const u32x4*)(ws + WS_H + ((size_t)(2 * tb) * 112 + 14 * w) * 1024) + lane;
  const u32x4* wp = (const u32x4*)(ws + WS_WD + ((size_t)(2 * db) * 112 + 14 * w) * 1024) + lane;
  const int t = tid >> 3, dq = tid & 7;
  const size_t oi = (size_t)(tb * 64 + t) * D_ + db * 64 + dq * 8;
  const u32x4 av0 = *(const u32x4*)(ws + WS_AT + (size_t)(db * 64 + t) * 4096 + tb * 128 + dq * 16);
  const u32x4 xv = *(const u32x4*)(x + oi);
  constexpr int PD = 8;
  u32x4 hb[PD][2], wb[PD][2];
#pragma unroll
  for (int s = 0; s < PD; ++s) {
    hb[s][0] = hp[s * 64]; hb[s][1] = hp[(112 + s) * 64];
    wb[s][0] = wp[s * 64]; wb[s][1] = wp[(112 + s) * 64];
  }
  f32x16 acc[2][2];
#pragma unroll
  for (int j = 0; j < 2; ++j)
#pragma unroll
    for (int i = 0; i < 2; ++i)
#pragma unroll
      for (int r = 0; r < 16; ++r) acc[j][i][r] = 0.f;
  {
    unsigned short* tl = (unsigned short*)(smem + AT_OFF);
#pragma unroll
    for (int e = 0; e < 4; ++e) {
      tl[(dq * 8 + 2 * e) * 72 + t] = (unsigned short)av0[e];
      tl[(dq * 8 + 2 * e + 1) * 72 + t] = (unsigned short)(av0[e] >> 16);
    }
  }
  __builtin_amdgcn_sched_barrier(0);
#pragma unroll
  for (int s = 0; s < 14; ++s) {
    const int p = s % PD;
#pragma unroll
    for (int j = 0; j < 2; ++j)
#pragma unroll
      for (int i = 0; i < 2; ++i)
        acc[j][i] = __builtin_amdgcn_mfma_f32_32x32x16_bf16(__builtin_bit_cast(bf16x8, wb[p][j]),
                                                            __builtin_bit_cast(bf16x8, hb[p][i]), acc[j][i], 0, 0, 0);
    if (s + PD < 14) {
      hb[p][0] = hp[(s + PD) * 64]; hb[p][1] = hp[(112 + s + PD) * 64];
      wb[p][0] = wp[(s + PD) * 64]; wb[p][1] = wp[(112 + s + PD) * 64];
    }
    __builtin_amdgcn_sched_barrier(0);
  }
  STAMP(2, 1);
  const unsigned rb = sb + w * 16384;
#pragma unroll
  for (int j = 0; j < 2; ++j)
#pragma unroll
    for (int i = 0; i < 2; ++i)
#pragma unroll
      for (int g = 0; g < 4; ++g) {
        const int tt = 32 * i + (lane & 31), un = 8 * j + 2 * g + (lane >> 5);
        f32x4 v = {acc[j][i][4 * g], acc[j][i][4 * g + 1], acc[j][i][4 * g + 2], acc[j][i][4 * g + 3]};
        *LDSP(f32x4, rb + tt * 256 + ((un ^ (tt & 15)) * 16)) = v;
      }
  BAR();
  STAMP(2, 2);
  f32x4 s0 = {0, 0, 0, 0}, s1 = {0, 0, 0, 0};
#pragma unroll
  for (int k = 0; k < 8; ++k) {
    const unsigned r2 = sb + k * 16384 + t * 256;
    s0 += *LDSP(f32x4, r2 + (((2 * dq) ^ (t & 15)) * 16));
    s1 += *LDSP(f32x4, r2 + (((2 * dq + 1) ^ (t & 15)) * 16));
  }
  const u32x4 av = *(const u32x4*)(smem + AT_OFF + t * 144 + dq * 16);
  u32x4 o;
  o[0] = pkbf(rbf(bflo(xv[0]) + bflo(av[0])) + rbf(s0[0]), rbf(bfhi(xv[0]) + bfhi(av[0])) + rbf(s0[1]));
  o[1] = pkbf(rbf(bflo(xv[1]) + bflo(av[1])) + rbf(s0[2]), rbf(bfhi(xv[1]) + bfhi(av[1])) + rbf(s0[3]));
  o[2] = pkbf(rbf(bflo(xv[2]) + bflo(av[2])) + rbf(s1[0]), rbf(bfhi(xv[2]) + bfhi(av[2])) + rbf(s1[1]));
  o[3] = pkbf(rbf(bflo(xv[3]) + bflo(av[3])) + rbf(s1[2]), rbf(bfhi(xv[3]) + bfhi(av[3])) + rbf(s1[3]));
  *(u32x4*)(out + oi) = o;
  STAMP(2, 3);
}

extern "C" void kernel_launch(void* const* d_in, const int* in_sizes, int n_in, void* d_out, int out_size, void* d_ws,
                              size_t ws_size, hipStream_t stream) {
  (void)in_sizes; (void)n_in; (void)out_size; (void)ws_size;
  (void)hipFuncSetAttribute((const void*)k0, hipFuncAttributeMaxDynamicSharedMemorySize, K0_LDS);
  (void)hipFuncSetAttribute((const void*)k1, hipFuncAttributeMaxDynamicSharedMemorySize, K1_LDS);
  (void)hipFuncSetAttribute((const void*)k2, hipFuncAttributeMaxDynamicSharedMemorySize, K2_LDS);
  typedef const unsigned short* P;
  k0<<<512, 256, K0_LDS, stream>>>((P)d_in[0], (P)d_in[1], (P)d_in[2], (P)d_in[3], (P)d_in[4], (P)d_in[5], (P)d_in[10],
                                   (char*)d_ws);
  k1<<<512, 256, K1_LDS, stream>>>((P)d_in[8], (P)d_in[9], (P)d_in[6], (char*)d_ws);
  k2<<<256, 512, K2_LDS, stream>>>((P)d_in[0], (char*)d_ws, (unsigned short*)d_out);
}
```

```cpp
#include <hip/hip_runtime.h>
#include <hip/hip_bf16.h>
#include <stdint.h>
#include <utility>

typedef __attribute__((ext_vector_type(8))) __bf16 bf16x8;
typedef __attribute__((ext_vector_type(2))) __bf16 bf16x2;
typedef __attribute__((ext_vector_type(8))) _Float16 f16x8;
typedef __attribute__((ext_vector_type(2))) _Float16 f16x2;
typedef __attribute__((ext_vector_type(4))) short s16x4;
typedef __attribute__((ext_vector_type(2))) float f32x2;
typedef __attribute__((ext_vector_type(4))) float f32x4;
typedef __attribute__((ext_vector_type(16))) float f32x16;
typedef __attribute__((ext_vector_type(4))) unsigned u32x4;
typedef __attribute__((ext_vector_type(2))) unsigned u32x2;
#define LDSP(T, a) ((__attribute__((address_space(3))) T*)(a))
#define GLB(T, a) ((__attribute__((address_space(1))) T*)(a))

constexpr int L_ = 2048, D_ = 512, F_ = 1792;
constexpr size_t WS_H = 0;
constexpr size_t WS_WD = WS_H + 64ull * 112 * 1024;
constexpr size_t WS_UT = WS_WD + 16ull * 112 * 1024;
constexpr size_t WS_KR = WS_UT + 512ull * 2048 * 2;
constexpr size_t WS_AT = WS_KR + 512ull * 2 * 2080 * 2;
constexpr size_t WS_KS = WS_AT + 512ull * 2048 * 2;
constexpr size_t WS_XN = WS_KS + 2048;
constexpr size_t WS_W = WS_XN + 64ull * 32 * 1024;

__device__ __forceinline__ float bflo(unsigned v) { return __uint_as_float(v << 16); }
__device__ __forceinline__ float bfhi(unsigned v) { return __uint_as_float(v & 0xffff0000u); }
__device__ __forceinline__ float bf1(unsigned short v) { return __uint_as_float((unsigned)v << 16); }
__device__ __forceinline__ unsigned pkbf(float a, float b) {
  f32x2 f = {a, b};
  return __builtin_bit_cast(unsigned, __builtin_convertvector(f, bf16x2));
}
__device__ __forceinline__ float rbf(float a) { return bflo(pkbf(a, a)); }
__device__ __forceinline__ unsigned pkh(float a, float b) {
  f32x2 f = {a, b};
  return __builtin_bit_cast(unsigned, __builtin_convertvector(f, f16x2));
}
__device__ __forceinline__ void glds16(const void* g, unsigned lds_dst) {
  unsigned keep;
  asm volatile("s_mov_b32 %0, m0\n\ts_mov_b32 m0, %2\n\ts_nop 0\n\tglobal_load_lds_dwordx4 %1, off\n\ts_mov_b32 m0, %0"
               : "=&s"(keep) : "v"(g), "s"(lds_dst) : "memory");
}
#define WAITV(n) asm volatile("s_waitcnt vmcnt(" #n ")" ::: "memory")
#define BAR() asm volatile("s_waitcnt lgkmcnt(0)\n\ts_barrier" ::: "memory")

#define STAMP(k, i)
#define STAMPR(k, i, o)
#define STAMP2(k, i)

__device__ __forceinline__ f32x2 cmul(f32x2 a, f32x2 b) {
  f32x2 ar = {a[0], a[0]}, ai = {a[1], a[1]}, bp = {-b[1], b[0]};
  return __builtin_elementwise_fma(ai, bp, ar * b);
}
__device__ __forceinline__ f32x2 cexpz(float zr, float zi, float p) {
  float e = __builtin_amdgcn_exp2f(zr * p * 1.44269504088896f);
  float th = __builtin_amdgcn_fractf(zi * p * 0.15915494309189535f);
  return f32x2{e * __builtin_amdgcn_cosf(th), e * __builtin_amdgcn_sinf(th)};
}
template <int C> __device__ __forceinline__ float dppf(float v) {
  return __int_as_float(__builtin_amdgcn_update_dpp(0, __float_as_int(v), C, 0xf, 0xf, true));
}
__device__ __forceinline__ float rdl(float v, int l) { return __int_as_float(__builtin_amdgcn_readlane(__float_as_int(v), l)); }
__device__ __forceinline__ float wsum(float v) {
  v += dppf<0xB1>(v); v += dppf<0x4E>(v); v += dppf<0x141>(v); v += dppf<0x140>(v);
  return (rdl(v, 0) + rdl(v, 16)) + (rdl(v, 32) + rdl(v, 48));
}
__device__ __forceinline__ float wmax(float v) {
  v = fmaxf(v, dppf<0xB1>(v)); v = fmaxf(v, dppf<0x4E>(v)); v = fmaxf(v, dppf<0x141>(v)); v = fmaxf(v, dppf<0x140>(v));
  return fmaxf(fmaxf(rdl(v, 0), rdl(v, 16)), fmaxf(rdl(v, 32), rdl(v, 48)));
}

__device__ __forceinline__ void touch(unsigned& r, const void* p) {
  asm volatile("global_load_dword %0, %1, off" : "+v"(r) : "v"(p));
}
#define GST(T, p, v) *(T*)(p) = v
#define KEEP(v) asm volatile("" ::"v"(v))

constexpr int KG_ROW = 528;
constexpr int KG_CH = 64 * KG_ROW;
constexpr int KT_OFF = 2 * KG_CH;
constexpr int K0_LDS = KT_OFF + 3072;

__global__ void __launch_bounds__(256, 2)
k0(const unsigned short* __restrict__ x, const unsigned short* __restrict__ lar, const unsigned short* __restrict__ aim,
   const unsigned short* __restrict__ cre, const unsigned short* __restrict__ cim, const unsigned short* __restrict__ ldt,
   const unsigned short* __restrict__ wg, const unsigned short* __restrict__ wu, const unsigned short* __restrict__ wd,
   char* __restrict__ ws) {
  extern __shared__ __attribute__((aligned(16))) char smem[];
  const int tid = threadIdx.x, lane = tid & 63, w = __builtin_amdgcn_readfirstlane(tid >> 6);
  const int bid = blockIdx.x & 255;
  STAMP(0, 0); STAMPR(0, 14, 0);
  if (blockIdx.x < 256) {
    const int ch = w >> 1, hw = w & 1, dch = 2 * bid + ch, n = tid & 127;
    const unsigned short p_lar = lar[dch * 128 + n], p_aim = aim[dch * 128 + n];
    const unsigned short p_cr = cre[dch * 128 + n], p_ci = cim[dch * 128 + n];
    const unsigned short p_cr2 = cre[dch * 128 + (n ^ 64)], p_ci2 = cim[dch * 128 + (n ^ 64)];
    const unsigned short p_dt = ldt[dch];
    int idx[8];
    {
      const int l0 = 2040 - 8 * tid;
      const int sh = max(24 - __clz(l0 | 1), 0), bias = sh ? (1 << (sh - 1)) - 1 : 0, om = sh ? 1 : 0;
#pragma unroll
      for (int j = 0; j < 8; ++j) {
        const int l = l0 + 7 - j;
        idx[j] = ((l + bias + ((l >> sh) & om)) >> sh) + 128 * sh;
        asm volatile("" : "+v"(idx[j]));
      }
    }
    const float dt = rbf(__expf(bf1(p_dt)));
    const float zr = -rbf(__expf(bf1(p_lar))) * dt, zi = bf1(p_aim) * dt;
    const float cr = bf1(p_cr) * dt, ci = bf1(p_ci) * dt;
    const float cm = wmax(fmaxf(fmaxf(fabsf(cr), fabsf(ci)), fmaxf(fabsf(bf1(p_cr2) * dt), fabsf(bf1(p_ci2) * dt))));
    const float inv = cm > 0.f ? 1.0f / cm : 0.f;
    if (n == 0) *(float*)(ws + WS_KS + dch * 4) = 2.0f * cm;
    {
      char* kg = smem + ch * KG_CH + n * 4;
      auto put = [&](int row, f32x2 v) { *(unsigned*)(kg + row * KG_ROW) = pkh(v[0], v[1]); };
      const f32x2 e1 = cexpz(zr, zi, 1.f), e8c = cexpz(zr, -zi, 8.f), e128 = cexpz(zr, zi, 128.f);
      f32x2 R[8];
      R[1] = e1;
#pragma unroll
      for (int r = 2; r < 8; ++r) R[r] = cmul(R[r - 1], e1);
      f32x2 W = {1.f, 0.f};
      put(48, W);
      W = e8c;
      put(49, W);
#pragma unroll
      for (int b = 2; b < 16; ++b) { W = cmul(W, e8c); put(48 + b, W); }
      f32x2 G = {cr * inv, ci * inv};
#pragma unroll
      for (int q = 0; q <= 16; ++q) {
        if (q < 2) {
          put(8 * q, G);
#pragma unroll
          for (int r = 1; r < 8; ++r) put(8 * q + r, cmul(G, R[r]));
        } else if (q < 4) {
          put(16 + 4 * (q - 2), G);
#pragma unroll
          for (int r = 1; r < 4; ++r) put(16 + 4 * (q - 2) + r, cmul(G, R[2 * r]));
        } else if (q < 8) {
          put(24 + 2 * (q - 4), G);
          put(24 + 2 * (q - 4) + 1, cmul(G, R[4]));
        } else {
          put(24 + q, G);
        }
        if (q < 16) G = cmul(G, e128);
      }
    }
    BAR();
    STAMP(0, 1);
    {
      const char* kq = smem + ch * KG_CH + (lane & 15) * KG_ROW + (lane >> 4) * 16;
      const int t0 = hw ? 2 : 0;
      f32x4 a0 = {0, 0, 0, 0}, a1 = {0, 0, 0, 0};
#pragma unroll
      for (int ks = 0; ks < 8; ++ks) {
        f16x8 bfr = *(const f16x8*)(kq + 48 * KG_ROW + ks * 64);
        f16x8 af0 = *(const f16x8*)(kq + t0 * 16 * KG_ROW + ks * 64);
        f16x8 af1 = *(const f16x8*)(kq + 16 * KG_ROW + ks * 64);
        a0 = __builtin_amdgcn_mfma_f32_16x16x32_f16(af0, bfr, a0, 0, 0, 0);
        a1 = __builtin_amdgcn_mfma_f32_16x16x32_f16(af1, bfr, a1, 0, 0, 0);
      }
      _Float16* T = (_Float16*)(smem + KT_OFF) + ch * 768;
      const int b = lane & 15;
      const int i0 = (lane >> 4) * 4;
      const int c0 = hw ? 512 + 16 * i0 + b : (i0 & 7) + 128 * (i0 >> 3) + 8 * b, d0 = hw ? 16 : 1;
      const int c1 = hw ? 704 + lane : i0 < 8 ? 256 + 64 * (i0 >> 2) + 4 * b : 384 + 32 * ((i0 - 8) >> 1) + 2 * b;
      const int d1 = hw ? 0 : i0 < 8 ? 2 : 32;
      const bool ok0 = !hw || i0 < 12;
#pragma unroll
      for (int r = 0; r < 4; ++r) {
        if (ok0) T[c0 + r * d0] = (_Float16)a0[r];
        T[c1 + (hw ? 0 : (r & 1)) + (r >> 1) * d1] = (_Float16)a1[r];
      }
    }
    BAR();
#pragma unroll
    for (int c2 = 0; c2 < 2; ++c2) {
      const unsigned short* Tc = (const unsigned short*)(smem + KT_OFF) + c2 * 768;
      unsigned v[8];
#pragma unroll
      for (int j = 0; j < 8; ++j) v[j] = Tc[idx[j]];
      u32x4 e = {v[0] | v[1] << 16, v[2] | v[3] << 16, v[4] | v[5] << 16, v[6] | v[7] << 16};
      GST(u32x4, ws + WS_KR + (size_t)(2 * bid + c2) * 4160 + tid * 16, e);
    }
    if (tid < 8) {
      u32x4 z = {0, 0, 0, 0};
      *(u32x4*)(ws + WS_KR + (size_t)(2 * bid + (tid >> 2)) * 4160 + 4096 + (tid & 3) * 16) = z;
    }
    STAMP(0, 2); STAMPR(0, 15, 0);
    return;
  }
  STAMP2(0, 0); STAMPR(0, 14, 4);
  const int r0 = bid * 8;
  unsigned tc = 0;
  if (w == 3 && lane < 56) touch(tc, (const char*)wd + (size_t)bid * 7168 + lane * 128);
  u32x4 xv[2];
#pragma unroll
  for (int i = 0; i < 2; ++i) xv[i] = *(const u32x4*)(x + (size_t)(r0 + 2 * w + i) * D_ + lane * 8);
  u32x4 wc[4];
  const int wk = 4 * (bid >> 1), wun = min(tid, 223), wfi = wun / 14;
#pragma unroll
  for (int i = 0; i < 4; ++i) wc[i] = *(const u32x4*)((const char*)((bid & 1) ? wu : wg) + (size_t)(wk + i) * (F_ * 2) + wun * 16);
#pragma unroll
  for (int i = 0; i < 2; ++i) {
    float f[8];
#pragma unroll
    for (int e = 0; e < 4; ++e) { f[2 * e] = bflo(xv[i][e]); f[2 * e + 1] = bfhi(xv[i][e]); }
    const float mean = wsum(((f[0] + f[1]) + (f[2] + f[3])) + ((f[4] + f[5]) + (f[6] + f[7]))) * (1.0f / 512.0f);
    float q = 0.f;
#pragma unroll
    for (int e = 0; e < 8; ++e) q = fmaf(f[e] - mean, f[e] - mean, q);
    const float var = wsum(q) * (1.0f / 512.0f);
    const float mb = rbf(mean), sd = rbf(sqrtf(rbf(rbf(var) + 1e-5f)));
    u32x4 o;
#pragma unroll
    for (int e = 0; e < 4; ++e) o[e] = pkbf(rbf(f[2 * e] - mb) / sd, rbf(f[2 * e + 1] - mb) / sd);
    *(u32x4*)(smem + (2 * w + i) * 1040 + lane * 16) = o;
  }
  if (tid < 224) {
    char* wo = ws + WS_W + (size_t)wfi * 229376 + (wk >> 6) * 28672 + (bid & 1) * 14336 + (wk & 63) * 224 + (wun - wfi * 14) * 16;
#pragma unroll
    for (int i = 0; i < 4; ++i) GST(u32x4, wo + i * 224, wc[i]);
  }
  STAMP2(0, 1);
  BAR();
#pragma unroll
  for (int i = 0; i < 2; ++i) {
    const int c = (tid >> 3) + 32 * i, r = tid & 7;
    const u32x4 o = *(const u32x4*)(smem + r * 1040 + c * 16);
    GST(u32x4, ws + WS_XN + ((size_t)(r0 >> 5) * 32 + (c >> 1)) * 1024 + ((r0 & 31) + r + 32 * (c & 1)) * 16, o);
  }
  KEEP(tc);
  STAMP2(0, 2); STAMPR(0, 15, 4);
}

constexpr int UP_OFF = 64 * 224 + 128;
constexpr int ST_BYTES = UP_OFF + 64 * 224;
constexpr int CV_K = 8 * 4160;
constexpr int CV_C = CV_K + 4608;
constexpr int K1_LDS = 2 * CV_C;

template <class F, int... I> __device__ __forceinline__ void static_for_(F&& f, std::integer_sequence<int, I...>) {
  (f(std::integral_constant<int, I>{}), ...);
}
template <int N, class F> __device__ __forceinline__ void static_for(F&& f) { static_for_(f, std::make_integer_sequence<int, N>{}); }
__host__ __device__ constexpr int seq_st(int g) { return g < 168 ? g / 28 : g < 192 ? 6 + (g >= 180) : 6 + ((g >> 2) & 1); }
__host__ __device__ constexpr int seq_s4(int g) { return g < 168 ? (g % 28) / 7 : g < 180 ? (g - 168) / 3 : g & 3; }
__host__ __device__ constexpr int seq_j(int g) { return g < 168 ? g % 7 : g < 180 ? (g - 168) % 3 : g < 192 ? (g - 180) >> 2 : (g - 168) / 8; }

__global__ void __launch_bounds__(256, 2)
k1(const unsigned short* __restrict__ dskip, const unsigned short* __restrict__ wd, char* __restrict__ ws) {
  extern __shared__ __attribute__((aligned(16))) char smem[];
  const unsigned sb = (unsigned)(uintptr_t)LDSP(char, smem);
  const int tid = threadIdx.x, lane = tid & 63, w = __builtin_amdgcn_readfirstlane(tid >> 6);
  const int bid = blockIdx.x & 255;
  if (blockIdx.x < 256) {
    STAMP(1, 0); STAMPR(1, 14, 0);
    const int xc = bid & 7, Lc = bid >> 3;
    const int mi = 8 * (xc & 1) + (Lc & 7), fi = 4 * (xc >> 1) + (Lc >> 3);
    const char* wbase = ws + WS_W + (size_t)fi * 229376 + w * 7168 + 3072;
    unsigned wl = lane * 16;
    const unsigned swa = sb + (w >> 1) * UP_OFF + ((w & 1) * 448 + lane) * 16;
    const char* xb = ws + WS_XN + (size_t)(mi * 4 + w) * 32768;
    u32x4 wr[2][7], xr[2][4];
    auto gload = [&](int st, int p, int i) __attribute__((always_inline)) { wr[p][i] = *(const u32x4*)(wbase + st * 28672 + (i - 3) * 1024 + wl); };
    auto xload = [&](int st, int p, int i) __attribute__((always_inline)) { xr[p][i] = *(const u32x4*)(xb + (st * 4 + i) * 1024 + lane * 16); };
    auto swrite = [&](int p, int i) __attribute__((always_inline)) { *LDSP(u32x4, swa + p * ST_BYTES + i * 1024) = wr[p][i]; };
#define SB0 __builtin_amdgcn_sched_barrier(0)
#pragma unroll
    for (int i = 0; i < 7; ++i) gload(0, 0, i);
    f32x16 acc[7];
#pragma unroll
    for (int p = 0; p < 2; ++p) {
      SB0;
#pragma unroll
      for (int i = 0; i < 7; ++i) {
        if (p) swrite(0, i);
        gload(p + 1, p ^ 1, i); SB0;
        if (i == 1) { xload(p, p, 0); SB0; }
        if (i == 4) { xload(p, p, 1); SB0; }
      }
      xload(p, p, 2); SB0; xload(p, p, 3); SB0;
      if (p == 0) {
#pragma unroll
        for (int j = 0; j < 7; ++j)
#pragma unroll
          for (int r = 0; r < 16; ++r) acc[j][r] = 0.f;
      }
    }
    const unsigned tra = sb + (8 * (lane >> 5) + ((lane >> 2) & 3)) * 224 + ((lane >> 4) & 1) * UP_OFF + (lane & 3) * 8;
    bf16x8 af[4];
    auto aread = [&](int g) __attribute__((always_inline)) {
      const unsigned o = (seq_st(g) & 1) * ST_BYTES + seq_s4(g) * 16 * 224 + seq_j(g) * 32;
      s16x4 lo = __builtin_amdgcn_ds_read_tr16_b64_v4i16(LDSP(s16x4, tra + o));
      s16x4 hi = __builtin_amdgcn_ds_read_tr16_b64_v4i16(LDSP(s16x4, tra + o + 4 * 224));
      af[g & 3] = __builtin_bit_cast(bf16x8, __builtin_shufflevector(lo, hi, 0, 1, 2, 3, 4, 5, 6, 7));
    };
    char* hp = ws + WS_H + ((size_t)(mi * 4 + w) * 112 + fi * 7) * 1024 + lane * 16;
    f32x2 ea[2], eb[2];
    u32x4 ho;
    auto epiA = [&](int e) __attribute__((always_inline)) {
      const int j = e >> 2, r = 2 * (e & 3);
      const f32x2 t = f32x2{acc[j][r], acc[j][r + 1]} * -1.44269504088896f;
      ea[e & 1] = f32x2{__builtin_amdgcn_exp2f(t[0]), __builtin_amdgcn_exp2f(t[1])};
    };
    auto epiB = [&](int e) __attribute__((always_inline)) {
      const f32x2 t = ea[e & 1] + 1.0f;
      eb[e & 1] = f32x2{__builtin_amdgcn_rcpf(t[0]), __builtin_amdgcn_rcpf(t[1])};
    };
    auto epiC = [&](int e) __attribute__((always_inline)) {
      const int j = e >> 2, r = 2 * (e & 3);
      const f32x2 t = f32x2{acc[j][r], acc[j][r + 1]} * f32x2{acc[j][r + 8], acc[j][r + 9]} * eb[e & 1];
      ho[e & 3] = pkbf(t[0], t[1]);
      if ((e & 3) == 3) GST(u32x4, hp + j * 1024, ho);
    };
    auto epi = [&](int q) __attribute__((always_inline)) {
      if (q & 1) { if (q < 56) epiB(q >> 1); }
      else { if (q < 56) epiA(q >> 1); if (q >= 2) epiC((q >> 1) - 1); }
    };
    wbase += 3 * 28672;
    xb += 8192;
    BAR();
    STAMP(1, 1);
    aread(0); aread(1); aread(2);
    __builtin_amdgcn_sched_barrier(0);
#pragma unroll 1
    for (int it = 0; it < 3; ++it) {
      const char* wb2 = it < 2 ? wbase + 28672 : wbase;
      const unsigned wl2 = it < 2 ? wl : 0u;
      static_for<56>([&](auto GI) __attribute__((always_inline)) {
        constexpr int g = decltype(GI)::value, so = g / 28, m = g % 28, s4 = m / 7, j = m % 7;
        if (m == 26) { BAR(); aread((g + 2) % 56); }
        if (m != 25) aread((g + 3) % 56);
        constexpr int wi = m % 3 == 1 ? m / 3 : -1;
        if constexpr (wi >= 0 && wi < 7) {
          swrite(so ^ 1, wi);
          wr[so ^ 1][wi] = *(const u32x4*)((so ? wb2 : wbase) + (wi - 3) * 1024 + (so ? wl2 : wl));
        }
        acc[j] = __builtin_amdgcn_mfma_f32_32x32x16_bf16(af[g & 3], __builtin_bit_cast(bf16x8, xr[so][s4]), acc[j], 0, 0, 0);
        if (j == 6) xload(so, so, s4);
        __builtin_amdgcn_sched_barrier(0);
      });
      wbase += 2 * 28672;
      xb += 8192;
      STAMP(1, 4 + it);
    }
    static_for<56>([&](auto GI) __attribute__((always_inline)) {
      constexpr int g = decltype(GI)::value + 168, st = seq_st(g), s4 = seq_s4(g), j = seq_j(g);
      if (g == 178) { BAR(); aread(g + 2); }
      if (g != 177 && g + 3 < 224) aread(g + 3);
      if (g < 175) swrite(1, g - 168);
      acc[j] = __builtin_amdgcn_mfma_f32_32x32x16_bf16(af[g & 3], __builtin_bit_cast(bf16x8, xr[st & 1][s4]), acc[j], 0, 0, 0);
      if (g >= 185) epi(g - 185);
      __builtin_amdgcn_sched_barrier(0);
    });
    STAMP(1, 2);
#pragma unroll
    for (int e = 39; e < 57; ++e) epi(e);
    STAMP(1, 3); STAMPR(1, 15, 0);
  } else {
    STAMP2(1, 0); STAMPR(1, 14, 4);
    const int xc = bid & 7, Lc = bid >> 3, cp = (xc * 8 + (Lc >> 2)) * 4 + (Lc & 3);
    const int ch = w >> 1, hw = w & 1, dch = 2 * cp + ch, n = tid & 127;
    const unsigned kb = sb + ch * CV_C, ub = kb + CV_K;
    const unsigned short p_dsk = dskip[dch];
    const float ksc = *(const float*)(ws + WS_KS + dch * 4);
    u32x4 kv[3];
#pragma unroll
    for (int i = 0; i < 3; ++i) kv[i] = *(const u32x4*)(ws + WS_KR + (size_t)dch * 4160 + n * 32 + i * 16);
    unsigned uv[8];
#pragma unroll
    for (int i = 0; i < 8; ++i)
      uv[i] = *(const unsigned*)(ws + WS_XN + ((size_t)(8 * i + (tid >> 5)) * 32 + (cp >> 3)) * 1024 +
                                 ((tid & 31) + 32 * ((cp >> 2) & 1)) * 16 + (cp & 3) * 4);
    unsigned wv[2][8];
#pragma unroll
    for (int i = 0; i < 2; ++i) {
      const int fr = bid * 7 + min(w + 4 * i, 6), dt = fr / 112, ks = fr - dt * 112;
      const unsigned short* s = wd + (size_t)(16 * ks + 4 * (lane >> 5)) * D_ + 32 * dt + (lane & 31);
#pragma unroll
      for (int j = 0; j < 8; ++j) wv[i][j] = s[(8 * (j >> 2) + (j & 3)) * D_];
    }
    STAMP2(1, 1);
    const float dsk = bf1(p_dsk);
    {
      unsigned E[12], O[11];
#pragma unroll
      for (int k = 0; k < 12; ++k) E[k] = kv[k >> 2][k & 3];
      u32x4 z = {0, 0, 0, 0};
#pragma unroll
      for (int i = 0; i < 8; ++i) {
        const unsigned h2 = pkh(bflo(uv[i]), bfhi(uv[i]));
        *LDSP(unsigned short, sb + CV_K + 480 + (tid + 256 * i) * 2) = (unsigned short)h2;
        *LDSP(unsigned short, sb + CV_C + CV_K + 480 + (tid + 256 * i) * 2) = (unsigned short)(h2 >> 16);
      }
      if (hw == 0 && lane < 30) *LDSP(u32x4, ub + lane * 16) = z;
      if (hw == 1 && lane < 8) *LDSP(u32x4, kb + lane * 4160 + 4096) = z;
#pragma unroll
      for (int k = 0; k < 11; ++k) O[k] = __builtin_amdgcn_alignbit(E[k + 1], E[k], 16);
#pragma unroll
      for (int c = 0; c < 8; ++c)
#pragma unroll
        for (int u = 0; u < 2; ++u) {
          const unsigned* q = (c & 1) ? O + 4 * u + (c >> 1) : E + 4 * u + (c >> 1);
          u32x4 v = {q[0], q[1], q[2], q[3]};
          *LDSP(u32x4, kb + c * 4160 + n * 32 + u * 16) = v;
        }
    }
    const int b = lane & 15, g = lane >> 4;
    unsigned aaddr = kb + 16 * ((7 - (b & 7)) * 260 + 253 - 224 + g - (b >> 3));
    unsigned baddr = ub + 16 * (28 + 2 * b + g);
    f32x4 acc[4];
#pragma unroll
    for (int i = 0; i < 4; ++i) acc[i] = f32x4{0, 0, 0, 0};
    BAR();
    STAMP2(1, 2);
#pragma unroll
    for (int i = 0; i < 2; ++i) {
      if (w + 4 * i < 7) {
        const unsigned* v = wv[i];
        u32x4 o = {v[0] | v[1] << 16, v[2] | v[3] << 16, v[4] | v[5] << 16, v[6] | v[7] << 16};
        GST(u32x4, ws + WS_WD + (size_t)(bid * 7 + w + 4 * i) * 1024 + lane * 16, o);
      }
    }
    auto conv = [&](auto HW) __attribute__((always_inline)) {
      constexpr int H = decltype(HW)::value, NB = 8 - H;
      f16x8 A[2][NB], B[2][NB];
      auto rd = [&](int p, int dq) __attribute__((always_inline)) {
#pragma unroll
        for (int m = 0; m < NB; ++m) {
          A[p][m] = *LDSP(f16x8, aaddr + 16 * (224 - 32 * m) + 64 - 64 * dq);
          B[p][m] = *LDSP(f16x8, baddr + 512 * m + 64 - 64 * dq);
        }
      };
      auto mm = [&](int p) __attribute__((always_inline)) {
#pragma unroll
        for (int m = 0; m < NB; ++m)
#pragma unroll
          for (int i = 0; i < 4; ++i) {
            const int T = (2 * i + (i & 1)) ^ H;
            if (m <= T) acc[i] = __builtin_amdgcn_mfma_f32_16x16x32_f16(A[p][m], B[p][T - m], acc[i], 0, 0, 0);
          }
#pragma unroll
        for (int k = 0; k < 2 * NB; ++k) {
          __builtin_amdgcn_sched_group_barrier(0x008, 1, 0);
          __builtin_amdgcn_sched_group_barrier(0x100, 1, 0);
        }
        SB0;
      };
      aaddr -= 64; baddr -= 64;
      rd(0, 0);
      SB0;
#pragma unroll 1
      for (int q2 = 0; q2 < 4; ++q2) {
        rd(1, 1);
        mm(0);
        if (q2 < 3) { aaddr -= 128; baddr -= 128; }
        rd(0, 0);
        mm(1);
      }
      STAMP2(1, 3);
      char* ap = ws + WS_AT + (size_t)dch * 4096 + b * 32 + g * 8;
#pragma unroll
      for (int i = 0; i < 4; ++i) {
        const int T = (2 * i + (i & 1)) ^ H;
        f32x4 y = acc[i] * ksc;
        u32x2 uu = *LDSP(u32x2, ub + 480 + T * 512 + b * 32 + g * 8);
        const unsigned uu0 = uu[0], uu1 = uu[1];
        f16x2 u01 = __builtin_bit_cast(f16x2, uu0), u23 = __builtin_bit_cast(f16x2, uu1);
        const unsigned y01 = pkbf(y[0], y[1]), y23 = pkbf(y[2], y[3]);
        const unsigned d01 = pkbf((float)u01[0] * dsk, (float)u01[1] * dsk), d23 = pkbf((float)u23[0] * dsk, (float)u23[1] * dsk);
        u32x2 pk = {pkbf(bflo(y01) + bflo(d01), bfhi(y01) + bfhi(d01)), pkbf(bflo(y23) + bflo(d23), bfhi(y23) + bfhi(d23))};
        GST(u32x2, ap + T * 512, pk);
      }
    };
    if (hw) conv(std::integral_constant<int, 1>{});
    else conv(std::integral_constant<int, 0>{});
    STAMP2(1, 4); STAMPR(1, 15, 4);
  }
}

constexpr int AT_OFF = 8 * 16384;
constexpr int K2_LDS = AT_OFF + 64 * 144;

__global__ void __launch_bounds__(512, 2)
k2(const unsigned short* __restrict__ x, char* __restrict__ ws, unsigned short* __restrict__ out) {
  extern __shared__ __attribute__((aligned(16))) char smem[];
  const unsigned sb = (unsigned)(uintptr_t)LDSP(char, smem);
  const int tid = threadIdx.x, lane = tid & 63, w = __builtin_amdgcn_readfirstlane(tid >> 6);
  const int bid = blockIdx.x, xc = bid & 7, Lc = bid >> 3;
  const int tb = 4 * xc + (Lc & 3), db = Lc >> 2;
  STAMP(2, 0); STAMPR(2, 14, 0);
  const u32x4* hp = (const u32x4*)(ws + WS_H + ((size_t)(2 * tb) * 112 + 14 * w) * 1024) + lane;
  const u32x4* wp = (const u32x4*)(ws + WS_WD + ((size_t)(2 * db) * 112 + 14 * w) * 1024) + lane;
  const int t = tid >> 3, dq = tid & 7;
  const size_t oi = (size_t)(tb * 64 + t) * D_ + db * 64 + dq * 8;
  const u32x4 av0 = *(const u32x4*)(ws + WS_AT + (size_t)(db * 64 + t) * 4096 + tb * 128 + dq * 16);
  const u32x4 xv = *(const u32x4*)(x + oi);
  constexpr int PD = 8;
  u32x4 hb[PD][2], wb[PD][2];
#pragma unroll
  for (int s = 0; s < PD; ++s) {
    hb[s][0] = hp[s * 64]; hb[s][1] = hp[(112 + s) * 64];
    wb[s][0] = wp[s * 64]; wb[s][1] = wp[(112 + s) * 64];
  }
  f32x16 acc[2][2];
#pragma unroll
  for (int j = 0; j < 2; ++j)
#pragma unroll
    for (int i = 0; i < 2; ++i)
#pragma unroll
      for (int r = 0; r < 16; ++r) acc[j][i][r] = 0.f;
  {
    unsigned short* tl = (unsigned short*)(smem + AT_OFF);
#pragma unroll
    for (int e = 0; e < 4; ++e) {
      tl[(dq * 8 + 2 * e) * 72 + t] = (unsigned short)av0[e];
      tl[(dq * 8 + 2 * e + 1) * 72 + t] = (unsigned short)(av0[e] >> 16);
    }
  }
  __builtin_amdgcn_sched_barrier(0);
#pragma unroll
  for (int s = 0; s < 14; ++s) {
    const int p = s % PD;
#pragma unroll
    for (int j = 0; j < 2; ++j)
#pragma unroll
      for (int i = 0; i < 2; ++i)
        acc[j][i] = __builtin_amdgcn_mfma_f32_32x32x16_bf16(__builtin_bit_cast(bf16x8, wb[p][j]),
                                                            __builtin_bit_cast(bf16x8, hb[p][i]), acc[j][i], 0, 0, 0);
    if (s + PD < 14) {
      hb[p][0] = hp[(s + PD) * 64]; hb[p][1] = hp[(112 + s + PD) * 64];
      wb[p][0] = wp[(s + PD) * 64]; wb[p][1] = wp[(112 + s + PD) * 64];
    }
    __builtin_amdgcn_sched_barrier(0);
  }
  STAMP(2, 1);
  const unsigned rb = sb + w * 16384;
#pragma unroll
  for (int j = 0; j < 2; ++j)
#pragma unroll
    for (int i = 0; i < 2; ++i)
#pragma unroll
      for (int g = 0; g < 4; ++g) {
        const int tt = 32 * i + (lane & 31), un = 8 * j + 2 * g + (lane >> 5);
        f32x4 v = {acc[j][i][4 * g], acc[j][i][4 * g + 1], acc[j][i][4 * g + 2], acc[j][i][4 * g + 3]};
        *LDSP(f32x4, rb + tt * 256 + ((un ^ (tt & 15)) * 16)) = v;
      }
  BAR();
  STAMP(2, 2);
  f32x4 s0 = {0, 0, 0, 0}, s1 = {0, 0, 0, 0};
#pragma unroll
  for (int k = 0; k < 8; ++k) {
    const unsigned r2 = sb + k * 16384 + t * 256;
    s0 += *LDSP(f32x4, r2 + (((2 * dq) ^ (t & 15)) * 16));
    s1 += *LDSP(f32x4, r2 + (((2 * dq + 1) ^ (t & 15)) * 16));
  }
  const u32x4 av = *(const u32x4*)(smem + AT_OFF + t * 144 + dq * 16);
  u32x4 o;
  o[0] = pkbf(rbf(bflo(xv[0]) + bflo(av[0])) + rbf(s0[0]), rbf(bfhi(xv[0]) + bfhi(av[0])) + rbf(s0[1]));
  o[1] = pkbf(rbf(bflo(xv[1]) + bflo(av[1])) + rbf(s0[2]), rbf(bfhi(xv[1]) + bfhi(av[1])) + rbf(s0[3]));
  o[2] = pkbf(rbf(bflo(xv[2]) + bflo(av[2])) + rbf(s1[0]), rbf(bfhi(xv[2]) + bfhi(av[2])) + rbf(s1[1]));
  o[3] = pkbf(rbf(bflo(xv[3]) + bflo(av[3])) + rbf(s1[2]), rbf(bfhi(xv[3]) + bfhi(av[3])) + rbf(s1[3]));
  *(u32x4*)(out + oi) = o;
  STAMP(2, 3); STAMPR(2, 15, 0);
}

extern "C" void kernel_launch(void* const* d_in, const int* in_sizes, int n_in, void* d_out, int out_size, void* d_ws,
                              size_t ws_size, hipStream_t stream) {
  (void)in_sizes; (void)n_in; (void)out_size; (void)ws_size;
  (void)hipFuncSetAttribute((const void*)k0, hipFuncAttributeMaxDynamicSharedMemorySize, K0_LDS);
  (void)hipFuncSetAttribute((const void*)k1, hipFuncAttributeMaxDynamicSharedMemorySize, K1_LDS);
  (void)hipFuncSetAttribute((const void*)k2, hipFuncAttributeMaxDynamicSharedMemorySize, K2_LDS);
  typedef const unsigned short* P;
  k0<<<512, 256, K0_LDS, stream>>>((P)d_in[0], (P)d_in[1], (P)d_in[2], (P)d_in[3], (P)d_in[4], (P)d_in[5], (P)d_in[8],
                                   (P)d_in[9], (P)d_in[10], (char*)d_ws);
  k1<<<512, 256, K1_LDS, stream>>>((P)d_in[6], (P)d_in[10], (char*)d_ws);
  k2<<<256, 512, K2_LDS, stream>>>((P)d_in[0], (char*)d_ws, (unsigned short*)d_out);
}
```

```cpp
#include <hip/hip_runtime.h>
#include <hip/hip_bf16.h>
#include <stdint.h>
#include <utility>

typedef __attribute__((ext_vector_type(8))) __bf16 bf16x8;
typedef __attribute__((ext_vector_type(2))) __bf16 bf16x2;
typedef __attribute__((ext_vector_type(8))) _Float16 f16x8;
typedef __attribute__((ext_vector_type(2))) _Float16 f16x2;
typedef __attribute__((ext_vector_type(4))) short s16x4;
typedef __attribute__((ext_vector_type(2))) float f32x2;
typedef __attribute__((ext_vector_type(4))) float f32x4;
typedef __attribute__((ext_vector_type(16))) float f32x16;
typedef __attribute__((ext_vector_type(4))) unsigned u32x4;
typedef __attribute__((ext_vector_type(2))) unsigned u32x2;
#define LDSP(T, a) ((__attribute__((address_space(3))) T*)(a))
#define GLB(T, a) ((__attribute__((address_space(1))) T*)(a))

constexpr int L_ = 2048, D_ = 512, F_ = 1792;
constexpr size_t WS_H = 0;
constexpr size_t WS_WD = WS_H + 64ull * 112 * 1024;
constexpr size_t WS_UT = WS_WD + 16ull * 112 * 1024;
constexpr size_t WS_KR = WS_UT + 512ull * 2048 * 2;
constexpr size_t WS_AT = WS_KR + 512ull * 2 * 2080 * 2;
constexpr size_t WS_KS = WS_AT + 512ull * 2048 * 2;
constexpr size_t WS_XN = WS_KS + 2048;
constexpr size_t WS_W = WS_XN + 64ull * 32 * 1024;

__device__ __forceinline__ float bflo(unsigned v) { return __uint_as_float(v << 16); }
__device__ __forceinline__ float bfhi(unsigned v) { return __uint_as_float(v & 0xffff0000u); }
__device__ __forceinline__ float bf1(unsigned short v) { return __uint_as_float((unsigned)v << 16); }
__device__ __forceinline__ unsigned pkbf(float a, float b) {
  f32x2 f = {a, b};
  return __builtin_bit_cast(unsigned, __builtin_convertvector(f, bf16x2));
}
__device__ __forceinline__ float rbf(float a) { return bflo(pkbf(a, a)); }
__device__ __forceinline__ unsigned pkh(float a, float b) {
  f32x2 f = {a, b};
  return __builtin_bit_cast(unsigned, __builtin_convertvector(f, f16x2));
}
__device__ __forceinline__ void glds16(const void* g, unsigned lds_base, int off) {
  unsigned keep;
  asm volatile("s_mov_b32 %0, m0\n\ts_add_u32 m0, %2, %3\n\ts_mov_b32 exec_hi, 0xffffff\n\tglobal_load_lds_dwordx4 %1, off\n\ts_mov_b32 exec_hi, -1\n\ts_mov_b32 m0, %0"
               : "=&s"(keep) : "v"(g), "s"(lds_base), "i"(off) : "memory", "scc");
}
#define WAITV(n) asm volatile("s_waitcnt vmcnt(" #n ")" ::: "memory")
#define BAR() asm volatile("s_waitcnt lgkmcnt(0)\n\ts_barrier" ::: "memory")

#define STAMP(k, i)
#define STAMPR(k, i, o)
#define STAMP2(k, i)

__device__ __forceinline__ f32x2 cmul(f32x2 a, f32x2 b) {
  f32x2 ar = {a[0], a[0]}, ai = {a[1], a[1]}, bp = {-b[1], b[0]};
  return __builtin_elementwise_fma(ai, bp, ar * b);
}
__device__ __forceinline__ f32x2 cexpz(float zr, float zi, float p) {
  float e = __builtin_amdgcn_exp2f(zr * p * 1.44269504088896f);
  float th = __builtin_amdgcn_fractf(zi * p * 0.15915494309189535f);
  return f32x2{e * __builtin_amdgcn_cosf(th), e * __builtin_amdgcn_sinf(th)};
}
template <int C> __device__ __forceinline__ float dppf(float v) {
  return __int_as_float(__builtin_amdgcn_update_dpp(0, __float_as_int(v), C, 0xf, 0xf, true));
}
__device__ __forceinline__ float rdl(float v, int l) { return __int_as_float(__builtin_amdgcn_readlane(__float_as_int(v), l)); }
__device__ __forceinline__ float wsum(float v) {
  v += dppf<0xB1>(v); v += dppf<0x4E>(v); v += dppf<0x141>(v); v += dppf<0x140>(v);
  return (rdl(v, 0) + rdl(v, 16)) + (rdl(v, 32) + rdl(v, 48));
}
__device__ __forceinline__ float wmax(float v) {
  v = fmaxf(v, dppf<0xB1>(v)); v = fmaxf(v, dppf<0x4E>(v)); v = fmaxf(v, dppf<0x141>(v)); v = fmaxf(v, dppf<0x140>(v));
  return fmaxf(fmaxf(rdl(v, 0), rdl(v, 16)), fmaxf(rdl(v, 32), rdl(v, 48)));
}

__device__ __forceinline__ void touch(unsigned& r, const void* p) {
  asm volatile("global_load_dword %0, %1, off" : "+v"(r) : "v"(p));
}
#define GST(T, p, v) *(T*)(p) = v
#define SB0 __builtin_amdgcn_sched_barrier(0)
#define KEEP(v) asm volatile("" ::"v"(v))

constexpr int KG_ROW = 528;
constexpr int KG_CH = 64 * KG_ROW;
constexpr int KT_OFF = 2 * KG_CH;
constexpr int K0_LDS = KT_OFF + 3072;

__global__ void __launch_bounds__(256, 2)
k0(const unsigned short* __restrict__ x, const unsigned short* __restrict__ lar, const unsigned short* __restrict__ aim,
   const unsigned short* __restrict__ cre, const unsigned short* __restrict__ cim, const unsigned short* __restrict__ ldt,
   const unsigned short* __restrict__ wg, const unsigned short* __restrict__ wu, const unsigned short* __restrict__ wd,
   char* __restrict__ ws) {
  extern __shared__ __attribute__((aligned(16))) char smem[];
  const int tid = threadIdx.x, lane = tid & 63, w = __builtin_amdgcn_readfirstlane(tid >> 6);
  const int bid = blockIdx.x & 255;
  STAMP(0, 0); STAMPR(0, 14, 0);
  if (blockIdx.x < 256) {
    const int ch = w >> 1, hw = w & 1, dch = 2 * bid + ch, n = tid & 127;
    const unsigned short p_lar = lar[dch * 128 + n], p_aim = aim[dch * 128 + n];
    const unsigned short p_cr = cre[dch * 128 + n], p_ci = cim[dch * 128 + n];
    const unsigned short p_cr2 = cre[dch * 128 + (n ^ 64)], p_ci2 = cim[dch * 128 + (n ^ 64)];
    const unsigned short p_dt = ldt[dch];
    int idx[8];
    {
      const int l0 = 2040 - 8 * tid;
      const int sh = max(24 - __clz(l0 | 1), 0), bias = sh ? (1 << (sh - 1)) - 1 : 0, om = sh ? 1 : 0;
#pragma unroll
      for (int j = 0; j < 8; ++j) {
        const int l = l0 + 7 - j;
        idx[j] = ((l + bias + ((l >> sh) & om)) >> sh) + 128 * sh;
        asm volatile("" : "+v"(idx[j]));
      }
    }
    const float dt = rbf(__expf(bf1(p_dt)));
    const float zr = -rbf(__expf(bf1(p_lar))) * dt, zi = bf1(p_aim) * dt;
    const float cr = bf1(p_cr) * dt, ci = bf1(p_ci) * dt;
    const float cm = wmax(fmaxf(fmaxf(fabsf(cr), fabsf(ci)), fmaxf(fabsf(bf1(p_cr2) * dt), fabsf(bf1(p_ci2) * dt))));
    const float inv = cm > 0.f ? 1.0f / cm : 0.f;
    if (n == 0) *(float*)(ws + WS_KS + dch * 4) = 2.0f * cm;
    {
      char* kg = smem + ch * KG_CH + n * 4;
      auto put = [&](int row, f32x2 v) { *(unsigned*)(kg + row * KG_ROW) = pkh(v[0], v[1]); };
      const f32x2 e1 = cexpz(zr, zi, 1.f), e8c = cexpz(zr, -zi, 8.f), e128 = cexpz(zr, zi, 128.f);
      f32x2 R[8];
      R[1] = e1;
#pragma unroll
      for (int r = 2; r < 8; ++r) R[r] = cmul(R[r - 1], e1);
      f32x2 W = {1.f, 0.f};
      put(48, W);
      W = e8c;
      put(49, W);
#pragma unroll
      for (int b = 2; b < 16; ++b) { W = cmul(W, e8c); put(48 + b, W); }
      f32x2 G = {cr * inv, ci * inv};
#pragma unroll
      for (int q = 0; q <= 16; ++q) {
        if (q < 2) {
          put(8 * q, G);
#pragma unroll
          for (int r = 1; r < 8; ++r) put(8 * q + r, cmul(G, R[r]));
        } else if (q < 4) {
          put(16 + 4 * (q - 2), G);
#pragma unroll
          for (int r = 1; r < 4; ++r) put(16 + 4 * (q - 2) + r, cmul(G, R[2 * r]));
        } else if (q < 8) {
          put(24 + 2 * (q - 4), G);
          put(24 + 2 * (q - 4) + 1, cmul(G, R[4]));
        } else {
          put(24 + q, G);
        }
        if (q < 16) G = cmul(G, e128);
      }
    }
    BAR();
    STAMP(0, 1);
    {
      const char* kq = smem + ch * KG_CH + (lane & 15) * KG_ROW + (lane >> 4) * 16;
      const int t0 = hw ? 2 : 0;
      f32x4 a0 = {0, 0, 0, 0}, a1 = {0, 0, 0, 0};
#pragma unroll
      for (int ks = 0; ks < 8; ++ks) {
        f16x8 bfr = *(const f16x8*)(kq + 48 * KG_ROW + ks * 64);
        f16x8 af0 = *(const f16x8*)(kq + t0 * 16 * KG_ROW + ks * 64);
        f16x8 af1 = *(const f16x8*)(kq + 16 * KG_ROW + ks * 64);
        a0 = __builtin_amdgcn_mfma_f32_16x16x32_f16(af0, bfr, a0, 0, 0, 0);
        a1 = __builtin_amdgcn_mfma_f32_16x16x32_f16(af1, bfr, a1, 0, 0, 0);
      }
      _Float16* T = (_Float16*)(smem + KT_OFF) + ch * 768;
      const int b = lane & 15;
      const int i0 = (lane >> 4) * 4;
      const int c0 = hw ? 512 + 16 * i0 + b : (i0 & 7) + 128 * (i0 >> 3) + 8 * b, d0 = hw ? 16 : 1;
      const int c1 = hw ? 704 + lane : i0 < 8 ? 256 + 64 * (i0 >> 2) + 4 * b : 384 + 32 * ((i0 - 8) >> 1) + 2 * b;
      const int d1 = hw ? 0 : i0 < 8 ? 2 : 32;
      const bool ok0 = !hw || i0 < 12;
#pragma unroll
      for (int r = 0; r < 4; ++r) {
        if (ok0) T[c0 + r * d0] = (_Float16)a0[r];
        T[c1 + (hw ? 0 : (r & 1)) + (r >> 1) * d1] = (_Float16)a1[r];
      }
    }
    BAR();
#pragma unroll
    for (int c2 = 0; c2 < 2; ++c2) {
      const unsigned short* Tc = (const unsigned short*)(smem + KT_OFF) + c2 * 768;
      unsigned v[8];
#pragma unroll
      for (int j = 0; j < 8; ++j) v[j] = Tc[idx[j]];
      u32x4 e = {v[0] | v[1] << 16, v[2] | v[3] << 16, v[4] | v[5] << 16, v[6] | v[7] << 16};
      GST(u32x4, ws + WS_KR + (size_t)(2 * bid + c2) * 4160 + tid * 16, e);
    }
    if (tid < 8) {
      u32x4 z = {0, 0, 0, 0};
      *(u32x4*)(ws + WS_KR + (size_t)(2 * bid + (tid >> 2)) * 4160 + 4096 + (tid & 3) * 16) = z;
    }
    STAMP(0, 2); STAMPR(0, 15, 0);
    return;
  }
  STAMP2(0, 0); STAMPR(0, 14, 4);
  const int r0 = bid * 8;
  unsigned tc = 0;
  if (w == 3 && lane < 56) touch(tc, (const char*)wd + (size_t)bid * 7168 + lane * 128);
  u32x4 xv[2];
#pragma unroll
  for (int i = 0; i < 2; ++i) xv[i] = *(const u32x4*)(x + (size_t)(r0 + 2 * w + i) * D_ + lane * 8);
  u32x4 wc[4];
  const int wk = 4 * (bid >> 1), wun = min(tid, 223), wfi = wun / 14;
#pragma unroll
  for (int i = 0; i < 4; ++i) wc[i] = *(const u32x4*)((const char*)((bid & 1) ? wu : wg) + (size_t)(wk + i) * (F_ * 2) + wun * 16);
#pragma unroll
  for (int i = 0; i < 2; ++i) {
    float f[8];
#pragma unroll
    for (int e = 0; e < 4; ++e) { f[2 * e] = bflo(xv[i][e]); f[2 * e + 1] = bfhi(xv[i][e]); }
    const float mean = wsum(((f[0] + f[1]) + (f[2] + f[3])) + ((f[4] + f[5]) + (f[6] + f[7]))) * (1.0f / 512.0f);
    float q = 0.f;
#pragma unroll
    for (int e = 0; e < 8; ++e) q = fmaf(f[e] - mean, f[e] - mean, q);
    const float var = wsum(q) * (1.0f / 512.0f);
    const float mb = rbf(mean), sd = rbf(sqrtf(rbf(rbf(var) + 1e-5f)));
    u32x4 o;
#pragma unroll
    for (int e = 0; e < 4; ++e) o[e] = pkbf(rbf(f[2 * e] - mb) / sd, rbf(f[2 * e + 1] - mb) / sd);
    *(u32x4*)(smem + (2 * w + i) * 1040 + lane * 16) = o;
  }
  if (tid < 224) {
    char* wo = ws + WS_W + (size_t)wfi * 229376 + (wk >> 6) * 28672 + (bid & 1) * 14336 + (wk & 63) * 224 + (wun - wfi * 14) * 16;
#pragma unroll
    for (int i = 0; i < 4; ++i) GST(u32x4, wo + i * 224, wc[i]);
  }
  STAMP2(0, 1);
  BAR();
#pragma unroll
  for (int i = 0; i < 2; ++i) {
    const int c = (tid >> 3) + 32 * i, r = tid & 7;
    const u32x4 o = *(const u32x4*)(smem + r * 1040 + c * 16);
    GST(u32x4, ws + WS_XN + ((size_t)(r0 >> 5) * 32 + (c >> 1)) * 1024 + ((r0 & 31) + r + 32 * (c & 1)) * 16, o);
  }
  KEEP(tc);
  STAMP2(0, 2); STAMPR(0, 15, 4);
}

constexpr int UP_OFF = 32 * 224 + 128;
constexpr int ST_BYTES = UP_OFF + 32 * 224;
constexpr int CV_K = 8 * 4160;
constexpr int CV_C = CV_K + 4608;
constexpr int K1_LDS = 2 * CV_C;

template <class F, int... I> __device__ __forceinline__ void static_for_(F&& f, std::integer_sequence<int, I...>) {
  (f(std::integral_constant<int, I>{}), ...);
}
template <int N, class F> __device__ __forceinline__ void static_for(F&& f) { static_for_(f, std::make_integer_sequence<int, N>{}); }
__host__ __device__ constexpr int seq_st(int g) { return g < 168 ? g / 28 : g < 192 ? 6 + (g >= 180) : 6 + ((g >> 2) & 1); }
__host__ __device__ constexpr int seq_s4(int g) { return g < 168 ? (g % 28) / 7 : g < 180 ? (g - 168) / 3 : g & 3; }
__host__ __device__ constexpr int seq_j(int g) { return g < 168 ? g % 7 : g < 180 ? (g - 168) % 3 : g < 192 ? (g - 180) >> 2 : (g - 168) / 8; }

__global__ void __launch_bounds__(256, 2)
k1(const unsigned short* __restrict__ dskip, const unsigned short* __restrict__ wd, char* __restrict__ ws) {
  extern __shared__ __attribute__((aligned(16))) char smem[];
  const unsigned sb = (unsigned)(uintptr_t)LDSP(char, smem);
  const int tid = threadIdx.x, lane = tid & 63, w = __builtin_amdgcn_readfirstlane(tid >> 6);
  const int bid = blockIdx.x & 255;
  if (blockIdx.x < 256) {
    STAMP(1, 0); STAMPR(1, 14, 0);
    const int xc = bid & 7, Lc = bid >> 3;
    const int mi = 8 * (xc & 1) + (Lc & 7), fi = 4 * (xc >> 1) + (Lc >> 3);
    const char* wp = ws + WS_W + (size_t)fi * 229376 + (w >> 1) * 14336 + (w & 1) * 3584 + lane * 16;
    const unsigned swa = __builtin_amdgcn_readfirstlane(sb + (w >> 1) * UP_OFF + (w & 1) * 3584);
    const char* xp = ws + WS_XN + (size_t)(mi * 4 + w) * 32768 + lane * 16;
    u32x4 xr[8];
    auto gl = [&](int h, int i) __attribute__((always_inline)) {
      glds16(wp + (h >> 1) * 28672 + (h & 1) * 7168 + i * 896, swa, (h & 3) * ST_BYTES + i * 896);
    };
    auto xload = [&](int i) __attribute__((always_inline)) {
      asm volatile("global_load_dwordx4 %0, %1, off" : "=v"(xr[i]) : "v"(xp + i * 1024));
    };
#pragma unroll
    for (int h = 0; h < 4; ++h) {
#pragma unroll
      for (int i = 0; i < 4; ++i) gl(h, i);
      xload(2 * h); xload(2 * h + 1);
    }
    f32x16 acc[7];
#pragma unroll
    for (int j = 0; j < 7; ++j)
#pragma unroll
      for (int r = 0; r < 16; ++r) acc[j][r] = 0.f;
    const unsigned tra = sb + (8 * (lane >> 5) + ((lane >> 2) & 3)) * 224 + ((lane >> 4) & 1) * UP_OFF + (lane & 3) * 8;
    bf16x8 af[4];
    auto aread = [&](int g) __attribute__((always_inline)) {
      const unsigned o = ((seq_st(g) & 1) * 2 + (seq_s4(g) >> 1)) * ST_BYTES + (seq_s4(g) & 1) * 16 * 224 + seq_j(g) * 32;
      s16x4 lo = __builtin_amdgcn_ds_read_tr16_b64_v4i16(LDSP(s16x4, tra + o));
      s16x4 hi = __builtin_amdgcn_ds_read_tr16_b64_v4i16(LDSP(s16x4, tra + o + 4 * 224));
      af[g & 3] = __builtin_bit_cast(bf16x8, __builtin_shufflevector(lo, hi, 0, 1, 2, 3, 4, 5, 6, 7));
    };
    char* hp = ws + WS_H + ((size_t)(mi * 4 + w) * 112 + fi * 7) * 1024 + lane * 16;
    f32x2 ea[2], eb[2];
    u32x4 ho;
    auto epiA = [&](int e) __attribute__((always_inline)) {
      const int j = e >> 2, r = 2 * (e & 3);
      const f32x2 t = f32x2{acc[j][r], acc[j][r + 1]} * -1.44269504088896f;
      ea[e & 1] = f32x2{__builtin_amdgcn_exp2f(t[0]), __builtin_amdgcn_exp2f(t[1])};
    };
    auto epiB = [&](int e) __attribute__((always_inline)) {
      const f32x2 t = ea[e & 1] + 1.0f;
      eb[e & 1] = f32x2{__builtin_amdgcn_rcpf(t[0]), __builtin_amdgcn_rcpf(t[1])};
    };
    auto epiC = [&](int e) __attribute__((always_inline)) {
      const int j = e >> 2, r = 2 * (e & 3);
      const f32x2 t = f32x2{acc[j][r], acc[j][r + 1]} * f32x2{acc[j][r + 8], acc[j][r + 9]} * eb[e & 1];
      ho[e & 3] = pkbf(t[0], t[1]);
      if ((e & 3) == 3) GST(u32x4, hp + j * 1024, ho);
    };
    auto epi = [&](int q) __attribute__((always_inline)) {
      if (q & 1) { if (q < 56) epiB(q >> 1); }
      else { if (q < 56) epiA(q >> 1); if (q >= 2) epiC((q >> 1) - 1); }
    };
    SB0;
    WAITV(18);
    BAR();
    STAMP(1, 1);
    aread(0); aread(1); aread(2);
    SB0;
#pragma unroll 1
    for (int it = 0; it < 3; ++it) {
      wp += 2 * 28672;
      xp += 8192;
      static_for<56>([&](auto GI) __attribute__((always_inline)) {
        constexpr int g = decltype(GI)::value, h = g / 14, m = g % 14, ks = g / 7, j = g % 7;
        if (m == 12) { WAITV(13); BAR(); aread((g + 2) % 56); }
        if (m != 11) aread((g + 3) % 56);
        if (m >= 12) { gl(h, 2 * (m - 12)); gl(h, 2 * (m - 12) + 1); }
        SB0;
        acc[j] = __builtin_amdgcn_mfma_f32_32x32x16_bf16(af[g & 3], __builtin_bit_cast(bf16x8, xr[ks]), acc[j], 0, 0, 0);
        SB0;
        if (j == 6) xload(ks);
        SB0;
      });
      STAMP(1, 4 + it);
    }
    static_for<56>([&](auto GI) __attribute__((always_inline)) {
      constexpr int g = decltype(GI)::value + 168, st = seq_st(g), s4 = seq_s4(g), j = seq_j(g);
      if (g == 172) { WAITV(12); BAR(); aread(g + 2); }
      if (g == 178) { WAITV(0); BAR(); aread(g + 2); }
      if (g != 171 && g != 177 && g + 3 < 224) aread(g + 3);
      SB0;
      acc[j] = __builtin_amdgcn_mfma_f32_32x32x16_bf16(af[g & 3], __builtin_bit_cast(bf16x8, xr[(st & 1) * 4 + s4]), acc[j], 0, 0, 0);
      if (g >= 185) epi(g - 185);
      SB0;
    });
    STAMP(1, 2);
#pragma unroll
    for (int e = 39; e < 57; ++e) epi(e);
    STAMP(1, 3); STAMPR(1, 15, 0);
  } else {
    STAMP2(1, 0); STAMPR(1, 14, 4);
    const int xc = bid & 7, Lc = bid >> 3, cp = (xc * 8 + (Lc >> 2)) * 4 + (Lc & 3);
    const int ch = w >> 1, hw = w & 1, dch = 2 * cp + ch, n = tid & 127;
    const unsigned kb = sb + ch * CV_C, ub = kb + CV_K;
    const unsigned short p_dsk = dskip[dch];
    const float ksc = *(const float*)(ws + WS_KS + dch * 4);
    u32x4 kv[3];
#pragma unroll
    for (int i = 0; i < 3; ++i) kv[i] = *(const u32x4*)(ws + WS_KR + (size_t)dch * 4160 + n * 32 + i * 16);
    unsigned uv[8];
#pragma unroll
    for (int i = 0; i < 8; ++i)
      uv[i] = *(const unsigned*)(ws + WS_XN + ((size_t)(8 * i + (tid >> 5)) * 32 + (cp >> 3)) * 1024 +
                                 ((tid & 31) + 32 * ((cp >> 2) & 1)) * 16 + (cp & 3) * 4);
    unsigned wv[2][8];
#pragma unroll
    for (int i = 0; i < 2; ++i) {
      const int fr = bid * 7 + min(w + 4 * i, 6), dt = fr / 112, ks = fr - dt * 112;
      const unsigned short* s = wd + (size_t)(16 * ks + 4 * (lane >> 5)) * D_ + 32 * dt + (lane & 31);
#pragma unroll
      for (int j = 0; j < 8; ++j) wv[i][j] = s[(8 * (j >> 2) + (j & 3)) * D_];
    }
    STAMP2(1, 1);
    const float dsk = bf1(p_dsk);
    {
      unsigned E[12], O[11];
#pragma unroll
      for (int k = 0; k < 12; ++k) E[k] = kv[k >> 2][k & 3];
      u32x4 z = {0, 0, 0, 0};
#pragma unroll
      for (int i = 0; i < 8; ++i) {
        const unsigned h2 = pkh(bflo(uv[i]), bfhi(uv[i]));
        *LDSP(unsigned short, sb + CV_K + 480 + (tid + 256 * i) * 2) = (unsigned short)h2;
        *LDSP(unsigned short, sb + CV_C + CV_K + 480 + (tid + 256 * i) * 2) = (unsigned short)(h2 >> 16);
      }
      if (hw == 0 && lane < 30) *LDSP(u32x4, ub + lane * 16) = z;
      if (hw == 1 && lane < 8) *LDSP(u32x4, kb + lane * 4160 + 4096) = z;
#pragma unroll
      for (int k = 0; k < 11; ++k) O[k] = __builtin_amdgcn_alignbit(E[k + 1], E[k], 16);
#pragma unroll
      for (int c = 0; c < 8; ++c)
#pragma unroll
        for (int u = 0; u < 2; ++u) {
          const unsigned* q = (c & 1) ? O + 4 * u + (c >> 1) : E + 4 * u + (c >> 1);
          u32x4 v = {q[0], q[1], q[2], q[3]};
          *LDSP(u32x4, kb + c * 4160 + n * 32 + u * 16) = v;
        }
    }
    const int b = lane & 15, g = lane >> 4;
    unsigned aaddr = kb + 16 * ((7 - (b & 7)) * 260 + 253 - 224 + g - (b >> 3));
    unsigned baddr = ub + 16 * (28 + 2 * b + g);
    f32x4 acc[4];
#pragma unroll
    for (int i = 0; i < 4; ++i) acc[i] = f32x4{0, 0, 0, 0};
    BAR();
    STAMP2(1, 2);
#pragma unroll
    for (int i = 0; i < 2; ++i) {
      if (w + 4 * i < 7) {
        const unsigned* v = wv[i];
        u32x4 o = {v[0] | v[1] << 16, v[2] | v[3] << 16, v[4] | v[5] << 16, v[6] | v[7] << 16};
        GST(u32x4, ws + WS_WD + (size_t)(bid * 7 + w + 4 * i) * 1024 + lane * 16, o);
      }
    }
    auto conv = [&](auto HW) __attribute__((always_inline)) {
      constexpr int H = decltype(HW)::value, NB = 8 - H;
      f16x8 A[2][NB], B[2][NB];
      auto rd = [&](int p, int dq) __attribute__((always_inline)) {
#pragma unroll
        for (int m = 0; m < NB; ++m) {
          A[p][m] = *LDSP(f16x8, aaddr + 16 * (224 - 32 * m) + 64 - 64 * dq);
          B[p][m] = *LDSP(f16x8, baddr + 512 * m + 64 - 64 * dq);
        }
      };
      auto mm = [&](int p) __attribute__((always_inline)) {
#pragma unroll
        for (int m = 0; m < NB; ++m)
#pragma unroll
          for (int i = 0; i < 4; ++i) {
            const int T = (2 * i + (i & 1)) ^ H;
            if (m <= T) acc[i] = __builtin_amdgcn_mfma_f32_16x16x32_f16(A[p][m], B[p][T - m], acc[i], 0, 0, 0);
          }
#pragma unroll
        for (int k = 0; k < 2 * NB; ++k) {
          __builtin_amdgcn_sched_group_barrier(0x008, 1, 0);
          __builtin_amdgcn_sched_group_barrier(0x100, 1, 0);
        }
        SB0;
      };
      aaddr -= 64; baddr -= 64;
      rd(0, 0);
      SB0;
#pragma unroll 1
      for (int q2 = 0; q2 < 4; ++q2) {
        rd(1, 1);
        mm(0);
        if (q2 < 3) { aaddr -= 128; baddr -= 128; }
        rd(0, 0);
        mm(1);
      }
      STAMP2(1, 3);
      char* ap = ws + WS_AT + (size_t)dch * 4096 + b * 32 + g * 8;
#pragma unroll
      for (int i = 0; i < 4; ++i) {
        const int T = (2 * i + (i & 1)) ^ H;
        f32x4 y = acc[i] * ksc;
        u32x2 uu = *LDSP(u32x2, ub + 480 + T * 512 + b * 32 + g * 8);
        const unsigned uu0 = uu[0], uu1 = uu[1];
        f16x2 u01 = __builtin_bit_cast(f16x2, uu0), u23 = __builtin_bit_cast(f16x2, uu1);
        const unsigned y01 = pkbf(y[0], y[1]), y23 = pkbf(y[2], y[3]);
        const unsigned d01 = pkbf((float)u01[0] * dsk, (float)u01[1] * dsk), d23 = pkbf((float)u23[0] * dsk, (float)u23[1] * dsk);
        u32x2 pk = {pkbf(bflo(y01) + bflo(d01), bfhi(y01) + bfhi(d01)), pkbf(bflo(y23) + bflo(d23), bfhi(y23) + bfhi(d23))};
        GST(u32x2, ap + T * 512, pk);
      }
    };
    if (hw) conv(std::integral_constant<int, 1>{});
    else conv(std::integral_constant<int, 0>{});
    STAMP2(1, 4); STAMPR(1, 15, 4);
  }
}

constexpr int AT_OFF = 8 * 16384;
constexpr int K2_LDS = AT_OFF + 64 * 144;

__global__ void __launch_bounds__(512, 2)
k2(const unsigned short* __restrict__ x, char* __restrict__ ws, unsigned short* __restrict__ out) {
  extern __shared__ __attribute__((aligned(16))) char smem[];
  const unsigned sb = (unsigned)(uintptr_t)LDSP(char, smem);
  const int tid = threadIdx.x, lane = tid & 63, w = __builtin_amdgcn_readfirstlane(tid >> 6);
  const int bid = blockIdx.x, xc = bid & 7, Lc = bid >> 3;
  const int tb = 4 * xc + (Lc & 3), db = Lc >> 2;
  STAMP(2, 0); STAMPR(2, 14, 0);
  const u32x4* hp = (const u32x4*)(ws + WS_H + ((size_t)(2 * tb) * 112 + 14 * w) * 1024) + lane;
  const u32x4* wp = (const u32x4*)(ws + WS_WD + ((size_t)(2 * db) * 112 + 14 * w) * 1024) + lane;
  const int t = tid >> 3, dq = tid & 7;
  const size_t oi = (size_t)(tb * 64 + t) * D_ + db * 64 + dq * 8;
  const u32x4 av0 = *(const u32x4*)(ws + WS_AT + (size_t)(db * 64 + t) * 4096 + tb * 128 + dq * 16);
  const u32x4 xv = *(const u32x4*)(x + oi);
  constexpr int PD = 8;
  u32x4 hb[PD][2], wb[PD][2];
#pragma unroll
  for (int s = 0; s < PD; ++s) {
    hb[s][0] = hp[s * 64]; hb[s][1] = hp[(112 + s) * 64];
    wb[s][0] = wp[s * 64]; wb[s][1] = wp[(112 + s) * 64];
  }
  f32x16 acc[2][2];
#pragma unroll
  for (int j = 0; j < 2; ++j)
#pragma unroll
    for (int i = 0; i < 2; ++i)
#pragma unroll
      for (int r = 0; r < 16; ++r) acc[j][i][r] = 0.f;
  {
    unsigned short* tl = (unsigned short*)(smem + AT_OFF);
#pragma unroll
    for (int e = 0; e < 4; ++e) {
      tl[(dq * 8 + 2 * e) * 72 + t] = (unsigned short)av0[e];
      tl[(dq * 8 + 2 * e + 1) * 72 + t] = (unsigned short)(av0[e] >> 16);
    }
  }
  __builtin_amdgcn_sched_barrier(0);
#pragma unroll
  for (int s = 0; s < 14; ++s) {
    const int p = s % PD;
#pragma unroll
    for (int j = 0; j < 2; ++j)
#pragma unroll
      for (int i = 0; i < 2; ++i)
        acc[j][i] = __builtin_amdgcn_mfma_f32_32x32x16_bf16(__builtin_bit_cast(bf16x8, wb[p][j]),
                                                            __builtin_bit_cast(bf16x8, hb[p][i]), acc[j][i], 0, 0, 0);
    if (s + PD < 14) {
      hb[p][0] = hp[(s + PD) * 64]; hb[p][1] = hp[(112 + s + PD) * 64];
      wb[p][0] = wp[(s + PD) * 64]; wb[p][1] = wp[(112 + s + PD) * 64];
    }
    __builtin_amdgcn_sched_barrier(0);
  }
  STAMP(2, 1);
  const unsigned rb = sb + w * 16384;
#pragma unroll
  for (int j = 0; j < 2; ++j)
#pragma unroll
    for (int i = 0; i < 2; ++i)
#pragma unroll
      for (int g = 0; g < 4; ++g) {
        const int tt = 32 * i + (lane & 31), un = 8 * j + 2 * g + (lane >> 5);
        f32x4 v = {acc[j][i][4 * g], acc[j][i][4 * g + 1], acc[j][i][4 * g + 2], acc[j][i][4 * g + 3]};
        *LDSP(f32x4, rb + tt * 256 + ((un ^ (tt & 15)) * 16)) = v;
      }
  BAR();
  STAMP(2, 2);
  f32x4 s0 = {0, 0, 0, 0}, s1 = {0, 0, 0, 0};
#pragma unroll
  for (int k = 0; k < 8; ++k) {
    const unsigned r2 = sb + k * 16384 + t * 256;
    s0 += *LDSP(f32x4, r2 + (((2 * dq) ^ (t & 15)) * 16));
    s1 += *LDSP(f32x4, r2 + (((2 * dq + 1) ^ (t & 15)) * 16));
  }
  const u32x4 av = *(const u32x4*)(smem + AT_OFF + t * 144 + dq * 16);
  u32x4 o;
  o[0] = pkbf(rbf(bflo(xv[0]) + bflo(av[0])) + rbf(s0[0]), rbf(bfhi(xv[0]) + bfhi(av[0])) + rbf(s0[1]));
  o[1] = pkbf(rbf(bflo(xv[1]) + bflo(av[1])) + rbf(s0[2]), rbf(bfhi(xv[1]) + bfhi(av[1])) + rbf(s0[3]));
  o[2] = pkbf(rbf(bflo(xv[2]) + bflo(av[2])) + rbf(s1[0]), rbf(bfhi(xv[2]) + bfhi(av[2])) + rbf(s1[1]));
  o[3] = pkbf(rbf(bflo(xv[3]) + bflo(av[3])) + rbf(s1[2]), rbf(bfhi(xv[3]) + bfhi(av[3])) + rbf(s1[3]));
  *(u32x4*)(out + oi) = o;
  STAMP(2, 3); STAMPR(2, 15, 0);
}

extern "C" void kernel_launch(void* const* d_in, const int* in_sizes, int n_in, void* d_out, int out_size, void* d_ws,
                              size_t ws_size, hipStream_t stream) {
  (void)in_sizes; (void)n_in; (void)out_size; (void)ws_size;
  (void)hipFuncSetAttribute((const void*)k0, hipFuncAttributeMaxDynamicSharedMemorySize, K0_LDS);
  (void)hipFuncSetAttribute((const void*)k1, hipFuncAttributeMaxDynamicSharedMemorySize, K1_LDS);
  (void)hipFuncSetAttribute((const void*)k2, hipFuncAttributeMaxDynamicSharedMemorySize, K2_LDS);
  typedef const unsigned short* P;
  k0<<<512, 256, K0_LDS, stream>>>((P)d_in[0], (P)d_in[1], (P)d_in[2], (P)d_in[3], (P)d_in[4], (P)d_in[5], (P)d_in[8],
                                   (P)d_in[9], (P)d_in[10], (char*)d_ws);
  k1<<<512, 256, K1_LDS, stream>>>((P)d_in[6], (P)d_in[10], (char*)d_ws);
  k2<<<256, 512, K2_LDS, stream>>>((P)d_in[0], (char*)d_ws, (unsigned short*)d_out);
}
```
